```python
import torch

def _softmax(x, axis=-1):
    return torch.softmax(x, dim=axis)
def _gelu(x):
    return torch.nn.functional.gelu(x, approximate="tanh")
def _silu(x):
    return torch.nn.functional.silu(x)
def _sigmoid(x):
    return torch.sigmoid(x)
def _layernorm(x, axis=-1, eps=1e-5):
    m = x.mean(dim=axis, keepdim=True)
    v = x.var(dim=axis, keepdim=True, unbiased=False)
    return (x - m) / torch.sqrt(v + eps)
def _scatter_add(tgt, idx, src, axis=0):
    return tgt.clone().index_add_(axis, idx, src)
def _segment_sum(src, seg, n):
    out = torch.zeros((n,) + src.shape[1:], dtype=src.dtype)
    return out.index_add_(0, seg, src)
def _pad(x, pw):
    flat = tuple(v for pair in reversed(pw) for v in pair)
    return torch.nn.functional.pad(x, flat)

kernel_name = 'sequential_preLN__sliding_block_cached__glu_gated_glu__decode'

def setup_inputs():

    _w = (lambda a: (lambda t: t.to(torch.bfloat16) if t.is_floating_point() else t)(torch.as_tensor(a)))
    def _A_setup():
        B, Sq, D = 1, 1, 640
        Hq, Hkv, Sk, Dh = 32, 32, 8192, 192
        assert Hq % Hkv == 0, (Hq, Hkv)
        return {
            "x": _w((lambda *s: torch.randn(*s, dtype=torch.bfloat16) * 0.05)(B, Sq, D)),
            "k_cache": _w((lambda *s: torch.randn(*s, dtype=torch.bfloat16) * 3.2)(B, Hkv, Sk, Dh)),
            "v_cache": _w((lambda *s: torch.randn(*s, dtype=torch.bfloat16) * 0.4)(B, Hkv, Sk, Dh)),
            "wq": _w((lambda *s: torch.randn(*s, dtype=torch.bfloat16) * 3.2)(D, Hq * Dh) * (D ** -0.5)),
            "wo": _w((lambda *s: torch.randn(*s, dtype=torch.bfloat16) * 0.4)(Hq * Dh, D) * ((Hq * Dh) ** -0.5)),
        }
    _A_in = _A_setup()

    def _B_setup():
        B, S, D, F = 1, 1, 640, 1536
        return {
            "x": _w((lambda *s: torch.randn(*s, dtype=torch.bfloat16) * 0.05)(B, S, D)),
            "w_gate": _w((lambda *s: torch.randn(*s, dtype=torch.bfloat16) * 0.05)(D, F)),
            "w_up": _w((lambda *s: torch.randn(*s, dtype=torch.bfloat16) * 0.05)(D, F)),
            "w_down": _w((lambda *s: torch.randn(*s, dtype=torch.bfloat16) * 0.05)(F, D)),
        }
    _B_in = _B_setup()

    return {
        "A_x": _A_in["x"],
        "A_k_cache": _A_in["k_cache"],
        "A_v_cache": _A_in["v_cache"],
        "A_wq": _A_in["wq"],
        "A_wo": _A_in["wo"],
        "B_x": _B_in["x"],
        "B_w_gate": _B_in["w_gate"],
        "B_w_up": _B_in["w_up"],
        "B_w_down": _B_in["w_down"],
    }

def reference(A_x, A_k_cache, A_v_cache, A_wq, A_wo, B_x, B_w_gate, B_w_up, B_w_down):
    def _A_ref(x, k_cache, v_cache, wq, wo):
        B, Sq, D = x.shape
        Hq, Dh = 32, 192
        Hkv, Sk = k_cache.shape[1], k_cache.shape[2]
        W = min(4096, max(256, Sk // 4))
        rep = Hq // Hkv
        q = (lambda x, *d: x.permute(*d))((x @ wq).reshape(B, Sq, Hq, Dh), 0, 2, 1, 3)
        k = (lambda x, n, axis: torch.repeat_interleave(x, n, dim=axis))(k_cache[:, :, Sk - W:, :], rep, axis=1)
        v = (lambda x, n, axis: torch.repeat_interleave(x, n, dim=axis))(v_cache[:, :, Sk - W:, :], rep, axis=1)
        scores = torch.einsum("bhqd,bhkd->bhqk", q, k) * (Dh ** -0.5)
        p = _softmax(scores, axis=-1)
        o = torch.einsum("bhqk,bhkd->bhqd", p, v)
        return (lambda x, *d: x.permute(*d))(o, 0, 2, 1, 3).reshape(B, Sq, Hq * Dh) @ wo

    def _B_ref(x, w_gate, w_up, w_down):
        g = torch.einsum("bsd,df->bsf", x, w_gate)
        u = torch.einsum("bsd,df->bsf", x, w_up)
        h = _sigmoid(g) * u
        return torch.einsum("bsf,fd->bsd", h, w_down)

    x = A_x
    h = x + _A_ref(_layernorm(x, axis=-1), A_k_cache, A_v_cache, A_wq, A_wo)
    h2 = h + _B_ref(_layernorm(h, axis=-1), B_w_gate, B_w_up, B_w_down)
    return h2
```

```cpp
#include <hip/hip_runtime.h>
#include <hip/hip_bf16.h>
#include <stdint.h>

#define MIMIC 0

typedef unsigned short u16;
typedef unsigned u32x4 __attribute__((ext_vector_type(4)));
typedef unsigned u32x2 __attribute__((ext_vector_type(2)));

constexpr int D = 640, H = 32, DH = 192, SK = 8192, W = 2048, F = 1536, HD = H * DH;

__device__ __forceinline__ float bf2f(u16 b) { return __uint_as_float(((unsigned)b) << 16); }
__device__ __forceinline__ float lo_f(unsigned u) { return __uint_as_float(u << 16); }
__device__ __forceinline__ float hi_f(unsigned u) { return __uint_as_float(u & 0xffff0000u); }
__device__ __forceinline__ float rbf(float f) {
  return f;
}
__device__ __forceinline__ float wave_sum(float v) {
#pragma unroll
  for (int o = 32; o > 0; o >>= 1) v += __shfl_xor(v, o);
  return v;
}
__device__ __forceinline__ float wave_max(float v) {
#pragma unroll
  for (int o = 32; o > 0; o >>= 1) v = fmaxf(v, __shfl_xor(v, o));
  return v;
}

__device__ void layernorm640(const float* xs, float* xn, int tid) {
  int lane = tid & 63;
  float s = 0.f;
  for (int i = lane; i < D; i += 64) s += xs[i];
  s = wave_sum(s);
  float mean = s * (1.0f / D);
  float q = 0.f;
  for (int i = lane; i < D; i += 64) { float d = xs[i] - mean; q += d * d; }
  q = wave_sum(q);
  float var = rbf(q * (1.0f / D));
  float m = rbf(mean);
  float den = rbf(sqrtf(rbf(var + 1e-5f)));
  for (int i = tid; i < D; i += blockDim.x) xn[i] = rbf(rbf(xs[i] - m) / den);
}

__global__ void __launch_bounds__(256) k_qproj(const u16* x, const u16* wq, float* q) {
  __shared__ float xs[D], xn[D], red[32][64];
  int tid = threadIdx.x;
  for (int i = tid; i < D; i += 256) xs[i] = bf2f(x[i]);
  __syncthreads();
  layernorm640(xs, xn, tid);
  __syncthreads();
  int kg = tid >> 3, c = tid & 7;
  int col0 = blockIdx.x * 64 + c * 8;
  float acc[8] = {0, 0, 0, 0, 0, 0, 0, 0};
#pragma unroll 4
  for (int k = kg; k < D; k += 32) {
    u32x4 w = *(const u32x4*)(wq + (size_t)k * HD + col0);
    float xv = xn[k];
#pragma unroll
    for (int j = 0; j < 4; ++j) { acc[2 * j] += xv * lo_f(w[j]); acc[2 * j + 1] += xv * hi_f(w[j]); }
  }
#pragma unroll
  for (int j = 0; j < 8; ++j) red[kg][c * 8 + j] = acc[j];
  __syncthreads();
  if (tid < 64) {
    float s = 0.f;
    for (int g = 0; g < 32; ++g) s += red[g][tid];
    q[blockIdx.x * 64 + tid] = rbf(s);
  }
}

__global__ void __launch_bounds__(256) k_attn(const float* q, const u16* kc, const u16* vc, float* part  ) {
  __shared__ float qs[DH], sc[256], red[8], oacc[8][DH];
  int s = blockIdx.x, h = blockIdx.y, tid = threadIdx.x, lane = tid & 63, wave = tid >> 6;
  if (tid < DH) qs[tid] = q[h * DH + tid];
  __syncthreads();
  const u16* kb = kc + ((size_t)h * SK + (SK - W) + s * 256) * DH;
  const u16* vb = vc + ((size_t)h * SK + (SK - W) + s * 256) * DH;
  int sub = lane & 7, kk = lane >> 3;
  float qr[24];
#pragma unroll
  for (int i = 0; i < 3; ++i)
#pragma unroll
    for (int j = 0; j < 8; ++j) qr[i * 8 + j] = qs[i * 64 + sub * 8 + j];
  for (int it = 0; it < 8; ++it) {
    int key = wave * 64 + it * 8 + kk;
    float d = 0.f;
#pragma unroll
    for (int i = 0; i < 3; ++i) {
      u32x4 w = *(const u32x4*)(kb + (size_t)key * DH + i * 64 + sub * 8);
#pragma unroll
      for (int j = 0; j < 4; ++j) { d += qr[i * 8 + 2 * j] * lo_f(w[j]); d += qr[i * 8 + 2 * j + 1] * hi_f(w[j]); }
    }
    d += __shfl_xor(d, 1); d += __shfl_xor(d, 2); d += __shfl_xor(d, 4);
    if (sub == 0) sc[key] = rbf(rbf(d) * 0.07216878364870323f);
  }
  __syncthreads();
  float v = sc[tid];
  float m = wave_max(v);
  if (lane == 0) red[wave] = m;
  __syncthreads();
  m = fmaxf(fmaxf(red[0], red[1]), fmaxf(red[2], red[3]));
  float p = __expf(v - m);
  float l = wave_sum(p);
  if (lane == 0) red[4 + wave] = l;
  sc[tid] = p;
  __syncthreads();
  l = red[4] + red[5] + red[6] + red[7];
  float acc[24];
#pragma unroll
  for (int i = 0; i < 24; ++i) acc[i] = 0.f;
  for (int it = 0; it < 8; ++it) {
    int key = wave * 64 + it * 8 + kk;
    float pk = sc[key];
#pragma unroll
    for (int i = 0; i < 3; ++i) {
      u32x4 w = *(const u32x4*)(vb + (size_t)key * DH + i * 64 + sub * 8);
#pragma unroll
      for (int j = 0; j < 4; ++j) { acc[i * 8 + 2 * j] += pk * lo_f(w[j]); acc[i * 8 + 2 * j + 1] += pk * hi_f(w[j]); }
    }
  }
#pragma unroll
  for (int i = 0; i < 24; ++i) { float a = acc[i]; a += __shfl_xor(a, 8); a += __shfl_xor(a, 16); a += __shfl_xor(a, 32); acc[i] = a; }
  if (kk == 0) {
#pragma unroll
    for (int i = 0; i < 3; ++i)
#pragma unroll
      for (int j = 0; j < 8; ++j) oacc[wave][i * 64 + sub * 8 + j] = acc[i * 8 + j];
  }
  __syncthreads();
  float* pp = part + ((size_t)h * 8 + s) * 194;
  if (tid < DH) pp[2 + tid] = oacc[0][tid] + oacc[1][tid] + oacc[2][tid] + oacc[3][tid];
  if (tid == 0) { pp[0] = m; pp[1] = l; }
}

__global__ void __launch_bounds__(256) k_oproj(const float* part, const u16* wo, float* ypart  ) {
  __shared__ float os[24];
  int h = blockIdx.x >> 3, sl = blockIdx.x & 7, tid = threadIdx.x;
  if (tid < 24) {
    const float* pp = part + (size_t)h * 8 * 194;
    float M = -INFINITY;
    for (int s = 0; s < 8; ++s) M = fmaxf(M, pp[s * 194]);
    float L = 0.f, o = 0.f;
    for (int s = 0; s < 8; ++s) { float w = __expf(pp[s * 194] - M); L += w * pp[s * 194 + 1]; o += w * pp[s * 194 + 2 + sl * 24 + tid]; }
    os[tid] = rbf(o / L);
  }
  __syncthreads();
  if (tid < 160) {
    float a0 = 0, a1 = 0, a2 = 0, a3 = 0;
#pragma unroll
    for (int r = 0; r < 24; ++r) {
      u32x2 w = *(const u32x2*)(wo + (size_t)(h * DH + sl * 24 + r) * D + tid * 4);
      float ov = os[r];
      a0 += ov * lo_f(w[0]); a1 += ov * hi_f(w[0]); a2 += ov * lo_f(w[1]); a3 += ov * hi_f(w[1]);
    }
    float4 o4 = make_float4(a0, a1, a2, a3);
    *(float4*)(ypart + (size_t)blockIdx.x * D + tid * 4) = o4;
  }
}

__global__ void __launch_bounds__(256) k_hred(const float* ypart, const u16* x, float* hbuf) {
  __shared__ float red[4][64];
  int tid = threadIdx.x, c = blockIdx.x * 64 + (tid & 63), g = tid >> 6;
  float s = 0.f;
  for (int i = g; i < 256; i += 4) s += ypart[(size_t)i * D + c];
  red[g][tid & 63] = s;
  __syncthreads();
  if (tid < 64) { float t = red[0][tid] + red[1][tid] + red[2][tid] + red[3][tid]; hbuf[c] = rbf(bf2f(x[c]) + rbf(t)); }
}

__global__ void __launch_bounds__(256) k_gateup(const float* hbuf, const u16* wg, const u16* wu, float* hh) {
  __shared__ float xs[D], xn[D], redg[64][32], redu[64][32];
  int tid = threadIdx.x;
  for (int i = tid; i < D; i += 256) xs[i] = hbuf[i];
  __syncthreads();
  layernorm640(xs, xn, tid);
  __syncthreads();
  int kg = tid >> 2, c = tid & 3;
  int col0 = blockIdx.x * 32 + c * 8;
  float ag[8] = {0, 0, 0, 0, 0, 0, 0, 0}, au[8] = {0, 0, 0, 0, 0, 0, 0, 0};
#pragma unroll
  for (int k = kg; k < D; k += 64) {
    u32x4 w = *(const u32x4*)(wg + (size_t)k * F + col0);
    u32x4 w2 = *(const u32x4*)(wu + (size_t)k * F + col0);
    float xv = xn[k];
#pragma unroll
    for (int j = 0; j < 4; ++j) { ag[2 * j] += xv * lo_f(w[j]); ag[2 * j + 1] += xv * hi_f(w[j]); au[2 * j] += xv * lo_f(w2[j]); au[2 * j + 1] += xv * hi_f(w2[j]); }
  }
#pragma unroll
  for (int j = 0; j < 8; ++j) { redg[kg][c * 8 + j] = ag[j]; redu[kg][c * 8 + j] = au[j]; }
  __syncthreads();
  if (tid < 32) {
    float g = 0.f, u = 0.f;
    for (int i = 0; i < 64; ++i) { g += redg[i][tid]; u += redu[i][tid]; }
    g = rbf(g); u = rbf(u);
    float sg = rbf(1.0f / (1.0f + __expf(-g)));
    hh[blockIdx.x * 32 + tid] = rbf(sg * u);
  }
}

__global__ void __launch_bounds__(256) k_down(const float* hh, const float* hbuf, const u16* wd, __hip_bfloat16* out) {
  __shared__ float red[128][16];
  int tid = threadIdx.x;
  int kg = tid >> 1, c = tid & 1;
  int col0 = blockIdx.x * 16 + c * 8;
  float acc[8] = {0, 0, 0, 0, 0, 0, 0, 0};
#pragma unroll
  for (int k = kg; k < F; k += 128) {
    u32x4 w = *(const u32x4*)(wd + (size_t)k * D + col0);
    float xv = hh[k];
#pragma unroll
    for (int j = 0; j < 4; ++j) { acc[2 * j] += xv * lo_f(w[j]); acc[2 * j + 1] += xv * hi_f(w[j]); }
  }
#pragma unroll
  for (int j = 0; j < 8; ++j) red[kg][c * 8 + j] = acc[j];
  __syncthreads();
  if (tid < 16) {
    float s = 0.f;
    for (int g = 0; g < 128; ++g) s += red[g][tid];
    int col = blockIdx.x * 16 + tid;
    out[col] = __float2bfloat16(hbuf[col] + rbf(s));
  }
}

extern "C" void kernel_launch(void* const* d_in, const int* in_sizes, int n_in,
                              void* d_out, int out_size, void* d_ws, size_t ws_size,
                              hipStream_t stream) {
  const u16* x = (const u16*)d_in[0];
  const u16* kc = (const u16*)d_in[1];
  const u16* vc = (const u16*)d_in[2];
  const u16* wq = (const u16*)d_in[3];
  const u16* wo = (const u16*)d_in[4];
  const u16* wg = (const u16*)d_in[6];
  const u16* wu = (const u16*)d_in[7];
  const u16* wd = (const u16*)d_in[8];
  float* ws = (float*)d_ws;
  float* q = ws;
  float* part = ws + 8192;
  float* ypart = ws + 65536;
  float* hbuf = ws + 65536 + 256 * 640;
  float* hh = hbuf + 1024;
  k_qproj<<<96, 256, 0, stream>>>(x, wq, q);
  k_attn<<<dim3(8, 32), 256, 0, stream>>>(q, kc, vc, part);
  k_oproj<<<256, 256, 0, stream>>>(part, wo, ypart);
  k_hred<<<10, 256, 0, stream>>>(ypart, x, hbuf);
  k_gateup<<<48, 256, 0, stream>>>(hbuf, wg, wu, hh);
  k_down<<<40, 256, 0, stream>>>(hh, hbuf, wd, (__hip_bfloat16*)d_out);
}
```

```cpp
#include <hip/hip_runtime.h>
#include <hip/hip_bf16.h>
#include <stdint.h>

#define STAMPS 0

typedef unsigned short u16;
typedef unsigned u32;
typedef unsigned long long u64;
typedef u32 u32x4 __attribute__((ext_vector_type(4)));
typedef u32 u32x2 __attribute__((ext_vector_type(2)));
typedef float f32x4 __attribute__((ext_vector_type(4)));
typedef float f32x2 __attribute__((ext_vector_type(2)));
typedef __bf16 bf16x8 __attribute__((ext_vector_type(8)));
typedef __bf16 bf16x2 __attribute__((ext_vector_type(2)));
#define GAS __attribute__((address_space(1)))
#define LAS __attribute__((address_space(3)))
#define RLX_AGENT __ATOMIC_RELAXED, __HIP_MEMORY_SCOPE_AGENT

constexpr int D = 640, H = 32, DH = 192, SK = 8192, W = 2048, F = 1536, HD = H * DH;
constexpr int NTHR = 768;
#define VGATE 18
#define KGATE 21
#define QSLEEP0 64
#define QSLEEP1 16
#define CODEPF 0
#define WO_EARLY 3
#define PSLEEP 1
#define LDD ldpl

constexpr int WS_EPOCH = 0;
constexpr int HS_REP = 2560;
constexpr int WS_HS = 4096;
constexpr int GU_REP = 12288;
constexpr int WS_GU = 32768;
constexpr int WS_YP = WS_GU + 8 * GU_REP;
constexpr int WS_QP = WS_YP + H * D * 8;
constexpr int OP_REC = 1664;
constexpr int WS_OP = WS_QP + H * 8 * DH * 8;
constexpr int WS_END = WS_OP + H * 8 * OP_REC;
constexpr int WS_STAMP = 2 << 20;
constexpr u32 SPIN_CAP = 1u << 18;

constexpr int L_CNT = 0;
constexpr int L_Q = 16;
constexpr int L_P = 112;
constexpr int L_ML = L_P + 256;
constexpr int L_O = L_ML + 8;
constexpr int L_T = L_O + 8 * 192;
constexpr int L_ST = L_T + 2048;
constexpr int L_END = L_ST + 16;

__device__ __forceinline__ float lo_f(u32 u) { return __uint_as_float(u << 16); }
__device__ __forceinline__ float hi_f(u32 u) { return __uint_as_float(u & 0xffff0000u); }
__device__ __forceinline__ u32 pk_bf16(float lo, float hi) {
  bf16x2 v = {(__bf16)lo, (__bf16)hi};
  return __builtin_bit_cast(u32, v);
}
__device__ __forceinline__ float rbf(float f) { return (float)(__bf16)f; }
__device__ __forceinline__ float dot2(u32 a, u32 b, float acc) {
  return __builtin_amdgcn_fdot2_f32_bf16(__builtin_bit_cast(bf16x2, a), __builtin_bit_cast(bf16x2, b), acc, false);
}
__device__ __forceinline__ f32x4 mfma(u32x4 a, u32x4 b, f32x4 c) {
  return __builtin_amdgcn_mfma_f32_16x16x32_bf16(__builtin_bit_cast(bf16x8, a), __builtin_bit_cast(bf16x8, b), c, 0, 0, 0);
}
template <int CTRL> __device__ __forceinline__ float dpp(float x) {
  return __builtin_bit_cast(float, __builtin_amdgcn_mov_dpp(__builtin_bit_cast(int, x), CTRL, 0xf, 0xf, true));
}
template <int CTRL> __device__ __forceinline__ u32 dppu(u32 x) {
  return (u32)__builtin_amdgcn_mov_dpp((int)x, CTRL, 0xf, 0xf, true);
}
constexpr int XOR1 = 0xB1, XOR2 = 0x4E, HMIR = 0x141, ROR8 = 0x128, ROR4 = 0x124;
__device__ __forceinline__ float sum8(float x) { x += dpp<XOR1>(x); x += dpp<XOR2>(x); x += dpp<HMIR>(x); return x; }
__device__ __forceinline__ float max8(float x) { x = fmaxf(x, dpp<XOR1>(x)); x = fmaxf(x, dpp<XOR2>(x)); x = fmaxf(x, dpp<HMIR>(x)); return x; }
__device__ __forceinline__ float tr32(float a, float b) {
  auto t = __builtin_amdgcn_permlane32_swap(__float_as_uint(a), __float_as_uint(b), false, false);
  return __uint_as_float(t[0]) + __uint_as_float(t[1]);
}
__device__ __forceinline__ float tr16(float a, float b) {
  auto t = __builtin_amdgcn_permlane16_swap(__float_as_uint(a), __float_as_uint(b), false, false);
  return __uint_as_float(t[0]) + __uint_as_float(t[1]);
}
__device__ __forceinline__ float xrow_sum(float x) { x = tr16(x, x); return tr32(x, x); }
__device__ __forceinline__ float xrow_max(float x) {
  auto s = __builtin_amdgcn_permlane16_swap(__float_as_uint(x), __float_as_uint(x), false, false);
  x = fmaxf(__uint_as_float(s[0]), __uint_as_float(s[1]));
  auto t = __builtin_amdgcn_permlane32_swap(__float_as_uint(x), __float_as_uint(x), false, false);
  return fmaxf(__uint_as_float(t[0]), __uint_as_float(t[1]));
}
__device__ __forceinline__ float sum64(float x) { x = sum8(x); x += dpp<ROR8>(x); return xrow_sum(x); }

__device__ __forceinline__ u32x4 ldnt(const GAS char* p) { return __builtin_nontemporal_load((const GAS u32x4*)p); }
__device__ __forceinline__ u32x4 ldpl(const GAS char* p) { return *(const GAS u32x4*)p; }
__device__ __forceinline__ u32x4 ld_sc1(__amdgpu_buffer_rsrc_t rs, int off) { return __builtin_amdgcn_raw_buffer_load_b128(rs, off, 0, 16); }
__device__ __forceinline__ u32x2 ld_sc1_64(__amdgpu_buffer_rsrc_t rs, int off) { return __builtin_amdgcn_raw_buffer_load_b64(rs, off, 0, 16); }
__device__ __forceinline__ void st_sc1(__amdgpu_buffer_rsrc_t rs, int off, u32x4 v) { __builtin_amdgcn_raw_buffer_store_b128(v, rs, off, 0, 16); }
__device__ __forceinline__ void st_sc1_64(__amdgpu_buffer_rsrc_t rs, int off, u32x2 v) { __builtin_amdgcn_raw_buffer_store_b64(v, rs, off, 0, 16); }
__device__ __forceinline__ u32 fu(float f) { return __float_as_uint(f); }
__device__ __forceinline__ float uf(u32 u) { return __uint_as_float(u); }

#define CBAR() asm volatile("" ::: "memory")
#define LGKM0() asm volatile("s_waitcnt lgkmcnt(0)" ::: "memory")
#define VM0() asm volatile("s_waitcnt vmcnt(0)" ::: "memory")
#define WG_BARRIER() asm volatile("s_waitcnt lgkmcnt(0)\n\ts_barrier" ::: "memory")
#define SCHED() __builtin_amdgcn_sched_barrier(0)

__device__ __forceinline__ void lds_wait_ge(volatile LAS u32* w, u32 n) {
#pragma nounroll
  for (u32 sp = 0; sp < SPIN_CAP; ++sp) {
    if (*w >= n) break;
    __builtin_amdgcn_s_sleep(1);
  }
  CBAR();
}
__device__ __forceinline__ void lds_inc(LAS u32* w, int lane) {
  LGKM0();
  if (lane == 0) __hip_atomic_fetch_add(w, 1u, __ATOMIC_RELAXED, __HIP_MEMORY_SCOPE_WORKGROUP);
  CBAR();
}

#define STAMP(i) do { } while (0)
#define STAMP_DECL do { } while (0)
#define STAMP_OUT() do { } while (0)

__global__ void __launch_bounds__(NTHR, 3)
fused_block(const u16* __restrict__ x_, const u16* __restrict__ kc_, const u16* __restrict__ vc_, const u16* __restrict__ wq_,
            const u16* __restrict__ wo_, const u16* __restrict__ wg_, const u16* __restrict__ wu_, const u16* __restrict__ wd_,
            u16* __restrict__ out_, unsigned char* ws_) {
  __shared__ __attribute__((aligned(16))) u32 smem[L_END];
  LAS u32* L = (LAS u32*)smem;
  volatile LAS u32* LV = (volatile LAS u32*)smem;
  const int tid = threadIdx.x, lane = tid & 63, wave = __builtin_amdgcn_readfirstlane(tid >> 6);
  const int b = blockIdx.x;
  const int xcd = b & 7, idx = b >> 3;
  const int h = xcd * 4 + (idx >> 3), s = idx & 7;
  GAS unsigned char* wsb = (GAS unsigned char*)ws_;
  const __amdgpu_buffer_rsrc_t rs = __builtin_amdgcn_make_buffer_rsrc((void*)ws_, 0, WS_END, 0x00020000);
  const int r8 = lane >> 3, c8 = lane & 7;
  STAMP_DECL;
  STAMP(0);
  u32x4 msk;
#pragma unroll
  for (int d = 0; d < 4; ++d) msk[d] = (d == (c8 >> 1)) ? ((c8 & 1) ? 0xffff0000u : 0x0000ffffu) : 0u;
  u32 tag;

  if (wave < 4) {
    __builtin_amdgcn_s_setprio(2);
    const GAS char* kb = (const GAS char*)kc_ + ((size_t)h * SK + (SK - W) + s * 256 + wave * 64) * (DH * 2) + r8 * 384 + c8 * 16;
    u32x4 kr[24];
#pragma unroll
    for (int i = 0; i < 6; ++i) kr[i] = ldnt(kb + (i / 3) * 3072 + (i % 3) * 128);
    SCHED();
    WG_BARRIER();
#pragma unroll
    for (int i = 6; i < KGATE; ++i) kr[i] = ldnt(kb + (i / 3) * 3072 + (i % 3) * 128);
    SCHED();
    lds_inc(L + L_CNT + 1, lane);
#pragma unroll
    for (int i = KGATE; i < 24; ++i) kr[i] = ldnt(kb + (i / 3) * 3072 + (i % 3) * 128);
    SCHED();
    STAMP(1);
    lds_wait_ge(LV + L_CNT + 0, 3);
    STAMP(2);
    tag = LV[L_CNT + 8];
    u32x4 qp[3];
#pragma unroll
    for (int t = 0; t < 3; ++t) qp[t] = *(const LAS u32x4*)(L + L_Q + t * 32 + c8 * 4);
    float sc[8];
#pragma unroll
    for (int g = 0; g < 8; ++g) {
      float d0 = 0.f, d1 = 0.f;
#pragma unroll
      for (int t = 0; t < 3; ++t) {
        d0 = dot2(kr[g * 3 + t][0], qp[t][0], d0); d1 = dot2(kr[g * 3 + t][1], qp[t][1], d1);
        d0 = dot2(kr[g * 3 + t][2], qp[t][2], d0); d1 = dot2(kr[g * 3 + t][3], qp[t][3], d1);
      }
      float d = sum8(d0 + d1);
      sc[g] = rbf(rbf(d) * 0.07216878364870323f);
    }
    float m = fmaxf(fmaxf(fmaxf(sc[0], sc[1]), fmaxf(sc[2], sc[3])), fmaxf(fmaxf(sc[4], sc[5]), fmaxf(sc[6], sc[7])));
    m = fmaxf(m, dpp<ROR8>(m));
    m = xrow_max(m);
    float l = 0.f;
    u32 pp[8];
#pragma unroll
    for (int g = 0; g < 8; ++g) {
      float p = __builtin_amdgcn_exp2f((sc[g] - m) * 1.4426950408889634f);
      l += p;
      pp[g] = pk_bf16(p, p);
    }
    l += dpp<ROR8>(l);
    l = xrow_sum(l);
    if (c8 == 0) {
      *(LAS u32x4*)(L + L_P + wave * 64 + r8 * 8) = u32x4{pp[0], pp[1], pp[2], pp[3]};
      *(LAS u32x4*)(L + L_P + wave * 64 + r8 * 8 + 4) = u32x4{pp[4], pp[5], pp[6], pp[7]};
    }
    if (lane == 0) { L[L_ML + wave] = fu(m); L[L_ML + 4 + wave] = fu(l); }
    LGKM0();
    if (lane == 0) LV[L_CNT + 4 + wave] = 1u;
  } else if (wave < 8) {
    __builtin_amdgcn_s_setprio(1);
    const int w = wave - 4;
    const GAS char* vb = (const GAS char*)vc_ + ((size_t)h * SK + (SK - W) + s * 256 + w * 64) * (DH * 2) + r8 * 384 + c8 * 16;
    WG_BARRIER();
    lds_wait_ge(LV + L_CNT + 1, 4);
    STAMP(1);
    u32x4 vr[24];
#pragma unroll
    for (int i = 0; i < VGATE; ++i) vr[i] = ldnt(vb + (i / 3) * 3072 + (i % 3) * 128);
    SCHED();
    lds_inc(L + L_CNT + 2, lane);
#pragma unroll
    for (int i = VGATE; i < 24; ++i) vr[i] = ldnt(vb + (i / 3) * 3072 + (i % 3) * 128);
    SCHED();
    STAMP(2);
    lds_wait_ge(LV + L_CNT + 4 + w, 1);
    STAMP(3);
    tag = LV[L_CNT + 8];
    u32x4 p0 = *(const LAS u32x4*)(L + L_P + w * 64 + r8 * 8), p1 = *(const LAS u32x4*)(L + L_P + w * 64 + r8 * 8 + 4);
    f32x4 acc[3] = {{0.f, 0.f, 0.f, 0.f}, {0.f, 0.f, 0.f, 0.f}, {0.f, 0.f, 0.f, 0.f}};
#pragma unroll
    for (int g = 0; g < 8; ++g) {
      const u32 pv = g < 4 ? p0[g & 3] : p1[g & 3];
      const u32x4 A = {pv & msk[0], pv & msk[1], pv & msk[2], pv & msk[3]};
#pragma unroll
      for (int t = 0; t < 3; ++t) acc[t] = mfma(A, vr[g * 3 + t], acc[t]);
    }
    if ((lane >> 5) == ((lane >> 3) & 1)) {
      LAS u32* o = L + L_O + (w * 2 + (lane >> 5)) * 192 + c8 * 8 + ((lane >> 4) & 1) * 4;
#pragma unroll
      for (int t = 0; t < 3; ++t) *(LAS f32x4*)(o + t * 64) = acc[t];
    }
    LGKM0();
    STAMP(4);
    u32 old = 0;
    if (lane == 0) old = __hip_atomic_fetch_add(L + L_CNT + 3, 1u, __ATOMIC_RELAXED, __HIP_MEMORY_SCOPE_WORKGROUP);
    old = __builtin_amdgcn_readfirstlane(old);
    CBAR();
    if (old == 3u) {
      const f32x4 mm = *(const LAS f32x4*)(L + L_ML), ll = *(const LAS f32x4*)(L + L_ML + 4);
      const int ln = lane < 48 ? lane : 47;
      f32x4 pt[8];
#pragma unroll
      for (int k = 0; k < 8; ++k) pt[k] = *(const LAS f32x4*)(L + L_O + k * 192 + ln * 4);
      const float M = fmaxf(fmaxf(mm[0], mm[1]), fmaxf(mm[2], mm[3]));
      float f[4];
#pragma unroll
      for (int k = 0; k < 4; ++k) f[k] = __builtin_amdgcn_exp2f((mm[k] - M) * 1.4426950408889634f);
      const float Ls = f[0] * ll[0] + f[1] * ll[1] + f[2] * ll[2] + f[3] * ll[3];
      f32x4 o = (pt[0] + pt[1]) * f[0] + (pt[2] + pt[3]) * f[1] + (pt[4] + pt[5]) * f[2] + (pt[6] + pt[7]) * f[3];
      if (lane >= 48) { o[0] = M; o[1] = Ls; }
      const int base = WS_OP + (h * 8 + s) * OP_REC + lane * 32;
      if (lane <= 48) st_sc1(rs, base, u32x4{fu(o[0]), tag, fu(o[1]), tag});
      if (lane < 48) st_sc1(rs, base + 16, u32x4{fu(o[2]), tag, fu(o[3]), tag});
      if (lane == 0) LV[L_CNT + 9] = 1u;
      STAMP(5);
    }
    STAMP_OUT();
    return;
  } else {
    const int a = wave - 8;
    __builtin_amdgcn_s_setprio(3);
    if (a < 3) {
      const GAS char* xb = (const GAS char*)x_;
      const u32x4 xa = ldpl(xb + lane * 16);
      const u32 xc = *(const GAS u32*)(xb + 1024 + lane * 4);
      const GAS u32* xr = (const GAS u32*)(xb + 160 * s + 20 * r8);
      u32 xo[5];
#pragma unroll
      for (int i = 0; i < 5; ++i) xo[i] = xr[i];
      const GAS char* wb = (const GAS char*)wq_ + (size_t)(80 * s + 10 * r8) * (HD * 2) + h * 384 + a * 128 + c8 * 16;
      u32x4 wr[10];
#pragma unroll
      for (int g = 0; g < 10; ++g) wr[g] = ldnt(wb + g * (HD * 2));
      u32 ep = __builtin_amdgcn_raw_buffer_load_b32(rs, WS_EPOCH, 0, 16);
      SCHED();
      WG_BARRIER();
      SCHED();
      float xv[10] = {lo_f(xa[0]), hi_f(xa[0]), lo_f(xa[1]), hi_f(xa[1]), lo_f(xa[2]), hi_f(xa[2]), lo_f(xa[3]), hi_f(xa[3]), lo_f(xc), hi_f(xc)};
      float sm = 0.f;
#pragma unroll
      for (int i = 0; i < 10; ++i) sm += xv[i];
      const float mean = sum64(sm) * (1.0f / D);
      float sq = 0.f;
#pragma unroll
      for (int i = 0; i < 10; ++i) { const float dd = xv[i] - mean; sq += dd * dd; }
      const float var = rbf(sum64(sq) * (1.0f / D));
      const float mb = rbf(mean);
      const float rinv = 1.0f / rbf(__builtin_sqrtf(rbf(var + 1e-5f)));
      f32x4 acc = {0.f, 0.f, 0.f, 0.f};
#pragma unroll
      for (int g = 0; g < 10; ++g) {
        const float xg = (g & 1) ? hi_f(xo[g >> 1]) : lo_f(xo[g >> 1]);
        const float xn = rbf(xg - mb) * rinv;
        const u32 pv = pk_bf16(xn, xn);
        const u32x4 A = {pv & msk[0], pv & msk[1], pv & msk[2], pv & msk[3]};
        acc = mfma(A, wr[g], acc);
      }
#pragma unroll
      for (int i = 0; i < 4; ++i) {
        const float up = dpp<ROR8>(acc[i]);
        auto t = __builtin_amdgcn_permlane32_swap(fu(up), fu(up), false, false);
        acc[i] += uf(t[1]);
      }
      SCHED();
      tag = ep + 1u;
      if (a == 0 && lane == 0) { LV[L_CNT + 8] = tag; LV[L_CNT + 11] = 1u; }
      STAMP(1);
      if (lane < 32 && (lane & 8) == 0) {
        const int base = WS_QP + (h * 8 + s) * (DH * 8) + (a * 64 + c8 * 8 + (lane >> 4) * 4) * 8;
        st_sc1(rs, base, u32x4{fu(acc[0]), tag, fu(acc[1]), tag});
        st_sc1(rs, base + 16, u32x4{fu(acc[2]), tag, fu(acc[3]), tag});
      }
      const int gb = WS_QP + (h * 8 + (lane >> 5)) * (DH * 8) + a * 512 + (lane & 31) * 16;
      u32x4 v[4];
      __builtin_amdgcn_s_sleep(QSLEEP0);
#pragma nounroll
      for (u32 sp = 0; sp < SPIN_CAP; ++sp) {
        CBAR();
#pragma unroll
        for (int i = 0; i < 4; ++i) v[i] = ld_sc1(rs, gb + i * (2 * DH * 8));
        bool ok = true;
#pragma unroll
        for (int i = 0; i < 4; ++i) ok &= (v[i][1] == tag) & (v[i][3] == tag);
        if (__all(ok)) break;
        __builtin_amdgcn_s_sleep(QSLEEP1);
      }
      float q0 = (uf(v[0][0]) + uf(v[1][0])) + (uf(v[2][0]) + uf(v[3][0]));
      float q1 = (uf(v[0][2]) + uf(v[1][2])) + (uf(v[2][2]) + uf(v[3][2]));
      q0 = tr32(q0, q0); q1 = tr32(q1, q1);
      if (lane < 32) L[L_Q + a * 32 + lane] = pk_bf16(q0, q1);
      lds_inc(L + L_CNT + 0, lane);
      STAMP(2);
    } else {
      if (lane < 16) L[L_CNT + lane] = 0u;
      WG_BARRIER();
      SCHED();
      lds_wait_ge(LV + L_CNT + 11, 1);
      tag = LV[L_CNT + 8];
    }
  }

  const int tw = wave < 4 ? wave : wave - 4;
  __builtin_amdgcn_s_setprio(0);
  if (s < 5 && WO_EARLY > 0) lds_wait_ge(LV + L_CNT + 2, 4); else lds_wait_ge(LV + L_CNT + 9, 1);
  STAMP(3);

  if (s < 5) {
    const int g4 = lane >> 4, z = lane & 15;
    const GAS char* wb = (const GAS char*)wo_ + (size_t)(h * DH + 24 * tw + 6 * g4) * (D * 2) + s * 256 + z * 16;
    u32x4 wr[6];
#pragma unroll
    for (int k = 0; k < 6; ++k) {
      if (k == WO_EARLY) { SCHED(); lds_wait_ge(LV + L_CNT + 9, 1); SCHED(); }
      wr[k] = ldnt(wb + k * (D * 2));
    }
    SCHED();
    lds_wait_ge(LV + L_CNT + 9, 1);
    STAMP(4);
    const int gb = WS_OP + (h * 8 + c8) * OP_REC + (24 * tw + 6 * g4 + 3 * (z >> 3)) * 8;
    const int mb = WS_OP + (h * 8 + c8) * OP_REC + DH * 8;
    u32x2 v[3];
    u32x4 ml;
    bool ok = false;
#pragma nounroll
    for (u32 sp = 0; sp < SPIN_CAP; ++sp) {
      CBAR();
      if (!ok) {
        ml = ld_sc1(rs, mb);
#pragma unroll
        for (int i = 0; i < 3; ++i) v[i] = ld_sc1_64(rs, gb + i * 8);
        ok = (ml[1] == tag) & (ml[3] == tag);
#pragma unroll
        for (int i = 0; i < 3; ++i) ok &= (v[i][1] == tag);
      }
      if (__all(ok)) break;
      __builtin_amdgcn_s_sleep(PSLEEP);
    }
    STAMP(5);
    const float ms = uf(ml[0]), ls = uf(ml[2]);
    const float M = max8(ms);
    const float f = __builtin_amdgcn_exp2f((ms - M) * 1.4426950408889634f);
    const float wgt = f * __builtin_amdgcn_rcpf(sum8(f * ls));
    f32x4 acc = {0.f, 0.f, 0.f, 0.f};
    const u32 cls0 = z < 8 ? 0xffffffffu : 0u;
    u32 ov[3];
#pragma unroll
    for (int k = 0; k < 3; ++k) {
      const float o = sum8(wgt * uf(v[k][0]));
      ov[k] = pk_bf16(o, o);
    }
#pragma unroll
    for (int k = 0; k < 6; ++k) {
      const u32 pv = k < 3 ? (ov[k] & cls0) : (ov[k - 3] & ~cls0);
      const u32x4 A = {pv & msk[0], pv & msk[1], pv & msk[2], pv & msk[3]};
      acc = mfma(A, wr[k], acc);
    }
    *(LAS f32x4*)(L + L_T + (tw * 2 + (g4 >> 1)) * 128 + z * 8 + (g4 & 1) * 4) = acc;
    WG_BARRIER();
    if (tw < 4) {
      const LAS u32* tp = L + L_T + g4 * 512 + tw * 32 + z * 2;
      f32x2 t = (*(const LAS f32x2*)(tp) + *(const LAS f32x2*)(tp + 128)) + (*(const LAS f32x2*)(tp + 256) + *(const LAS f32x2*)(tp + 384));
      t[0] = xrow_sum(t[0]); t[1] = xrow_sum(t[1]);
      if (lane < 16) st_sc1(rs, WS_YP + (h * D + s * 128 + tw * 32 + z * 2) * 8, u32x4{fu(t[0]), tag, fu(t[1]), tag});
    }
    STAMP(6);
    STAMP_OUT();
    return;
  }

  const int fi = (idx >> 3) * 3 + (s - 5);
  if (fi < 6) {
    const int ft = xcd * 6 + fi, mat = ft & 1, tile = ft >> 1;
    u32 gw[5][8];
    {
      const GAS char* wb = (const GAS char*)(mat ? wu_ : wg_) + (size_t)(80 * tw + 10 * r8) * (F * 2) + tile * 128 + c8 * 16;
      u32x4 ta[5], tb[5];
#pragma unroll
      for (int i = 0; i < 5; ++i) { ta[i] = ldnt(wb + (2 * i) * (F * 2)); tb[i] = ldnt(wb + (2 * i + 1) * (F * 2)); }
      SCHED();
#pragma unroll
      for (int i = 0; i < 5; ++i)
#pragma unroll
        for (int d = 0; d < 4; ++d) {
          gw[i][2 * d] = __builtin_amdgcn_perm(tb[i][d], ta[i][d], 0x05040100u);
          gw[i][2 * d + 1] = __builtin_amdgcn_perm(tb[i][d], ta[i][d], 0x07060302u);
        }
#pragma unroll
      for (int i = 0; i < 5; ++i)
#pragma unroll
        for (int c = 0; c < 8; ++c) asm volatile("" : "+v"(gw[i][c]));
    }
    STAMP(7);
    u32x2 hv;
    {
      const int o0 = WS_HS + xcd * HS_REP + (40 * tw + 5 * r8 + (c8 < 5 ? c8 : 0)) * 8;
#pragma nounroll
      for (u32 sp = 0; sp < SPIN_CAP; ++sp) {
        CBAR();
        hv = ld_sc1_64(rs, o0);
        if (__all(hv[1] == tag)) break;
        __builtin_amdgcn_s_sleep(PSLEEP);
      }
    }
    STAMP(8);
    const float h0 = lo_f(hv[0]), h1 = hi_f(hv[0]);
    const float e5 = c8 < 5 ? 1.0f : 0.0f;
    const float wmean = sum64(e5 * (h0 + h1)) * (1.0f / 80.0f);
    const float wm2 = sum64(e5 * ((h0 - wmean) * (h0 - wmean) + (h1 - wmean) * (h1 - wmean)));
    if (lane == 0) { L[L_ST + tw] = fu(wmean); L[L_ST + 8 + tw] = fu(wm2); }
    WG_BARRIER();
    u32 hn[5];
    {
      const f32x4 ma = *(const LAS f32x4*)(L + L_ST), mbb = *(const LAS f32x4*)(L + L_ST + 4);
      const f32x4 qa = *(const LAS f32x4*)(L + L_ST + 8), qb = *(const LAS f32x4*)(L + L_ST + 12);
      const f32x4 ms4 = ma + mbb;
      const float mean = ((ms4[0] + ms4[1]) + (ms4[2] + ms4[3])) * 0.125f;
      const f32x4 da = ma - mean, db = mbb - mean;
      const f32x4 t4 = (qa + qb) + 80.0f * (da * da + db * db);
      const float var = rbf(((t4[0] + t4[1]) + (t4[2] + t4[3])) * (1.0f / D));
      const float mb = rbf(mean);
      const float rinv = __builtin_amdgcn_rcpf(rbf(__builtin_amdgcn_sqrtf(rbf(var + 1e-5f))));
      const u32 n0 = pk_bf16(rbf(h0 - mb) * rinv, rbf(h1 - mb) * rinv);
      hn[0] = __builtin_amdgcn_ds_swizzle(n0, 0x18 | (0 << 5));
      hn[1] = __builtin_amdgcn_ds_swizzle(n0, 0x18 | (1 << 5));
      hn[2] = __builtin_amdgcn_ds_swizzle(n0, 0x18 | (2 << 5));
      hn[3] = __builtin_amdgcn_ds_swizzle(n0, 0x18 | (3 << 5));
      hn[4] = __builtin_amdgcn_ds_swizzle(n0, 0x18 | (4 << 5));
    }
    {
      float ac[8];
#pragma unroll
      for (int c = 0; c < 8; ++c) ac[c] = 0.f;
#pragma unroll
      for (int i = 0; i < 5; ++i)
#pragma unroll
        for (int c = 0; c < 8; ++c) ac[c] = dot2(gw[i][c], hn[i], ac[c]);
      float b4[4], b2[2];
#pragma unroll
      for (int c = 0; c < 4; ++c) b4[c] = tr32(ac[c], ac[c + 4]);
#pragma unroll
      for (int c = 0; c < 2; ++c) b2[c] = tr16(b4[c], b4[c + 2]);
#pragma unroll
      for (int c = 0; c < 2; ++c) b2[c] += dpp<ROR8>(b2[c]);
      if ((lane & 8) == 0) {
        const int col = c8 * 8 + (lane >> 5) * 4 + ((lane >> 4) & 1) * 2;
        *(LAS f32x2*)(L + L_T + tw * 64 + col) = f32x2{b2[0], b2[1]};
      }
    }
    WG_BARRIER();
    if (tw < 2) {
      const int z = lane & 15;
      f32x4 t = *(const LAS f32x4*)(L + L_T + z * 4);
#pragma unroll
      for (int k = 1; k < 8; ++k) t += *(const LAS f32x4*)(L + L_T + k * 64 + z * 4);
      st_sc1(rs, WS_GU + (tw * 4 + (lane >> 4)) * GU_REP + mat * 6144 + tile * 256 + z * 16, u32x4{pk_bf16(t[0], t[1]), tag, pk_bf16(t[2], t[3]), tag});
    }
    STAMP(9);
    STAMP_OUT();
    return;
  }
  if (fi == 11) {
    if (tw < 5) {
      const int ru = xcd * 5 + tw;
      const u32 xp = *(const GAS u32*)((const GAS char*)x_ + (ru * 8 + c8) * 4);
      const int gb = WS_YP + (r8 * D + ru * 16 + c8 * 2) * 8;
      u32x4 v[4];
      bool ok[4] = {false, false, false, false};
      STAMP(13);
#pragma nounroll
      for (u32 sp = 0; sp < SPIN_CAP; ++sp) {
        CBAR();
#pragma unroll
        for (int i = 0; i < 4; ++i) if (!ok[i]) v[i] = ld_sc1(rs, gb + i * (8 * D * 8));
#pragma unroll
        for (int i = 0; i < 4; ++i) ok[i] = (v[i][1] == tag) & (v[i][3] == tag);
        if (__all(ok[0] & ok[1] & ok[2] & ok[3])) break;
        __builtin_amdgcn_s_sleep(PSLEEP);
      }
      STAMP(14);
      float y0 = (uf(v[0][0]) + uf(v[1][0])) + (uf(v[2][0]) + uf(v[3][0]));
      float y1 = (uf(v[0][2]) + uf(v[1][2])) + (uf(v[2][2]) + uf(v[3][2]));
      y0 += dpp<ROR8>(y0); y1 += dpp<ROR8>(y1);
      y0 = xrow_sum(y0); y1 = xrow_sum(y1);
      const u32 a0 = pk_bf16(y0, y1);
      const u32 hp = pk_bf16(lo_f(xp) + lo_f(a0), hi_f(xp) + hi_f(a0));
      st_sc1_64(rs, WS_HS + r8 * HS_REP + (ru * 8 + c8) * 8, u32x2{hp, tag});
      STAMP(15);
    }
    STAMP_OUT();
    return;
  }
  {
    const int dc = xcd * 5 + (fi - 6);
    const int pr = lane >> 1, ch = lane & 1;
    u32 dw[3][8];
    {
      const GAS char* wb = (const GAS char*)wd_ + (size_t)(192 * tw + 2 * pr) * (D * 2) + dc * 32 + ch * 16;
      u32x4 da[3], db[3];
#pragma unroll
      for (int i = 0; i < 3; ++i) { da[i] = LDD(wb + (i * 64) * (D * 2)); db[i] = LDD(wb + (i * 64 + 1) * (D * 2)); }
      SCHED();
#pragma unroll
      for (int i = 0; i < 3; ++i)
#pragma unroll
        for (int d = 0; d < 4; ++d) {
          dw[i][2 * d] = __builtin_amdgcn_perm(db[i][d], da[i][d], 0x05040100u);
          dw[i][2 * d + 1] = __builtin_amdgcn_perm(db[i][d], da[i][d], 0x07060302u);
        }
#pragma unroll
      for (int i = 0; i < 3; ++i)
#pragma unroll
        for (int c = 0; c < 8; ++c) asm volatile("" : "+v"(dw[i][c]));
    }
    STAMP(10);
    u32x2 g0, g1, u0, u1, hv;
    {
      const int o0 = WS_GU + xcd * GU_REP + (96 * tw + 32 * ch + pr) * 8, o1 = WS_GU + xcd * GU_REP + (96 * tw + 64 + pr) * 8;
      const int oh = WS_HS + xcd * HS_REP + (dc * 8 + c8) * 8;
#pragma nounroll
      for (u32 sp = 0; sp < SPIN_CAP; ++sp) {
        CBAR();
        g0 = ld_sc1_64(rs, o0); u0 = ld_sc1_64(rs, o0 + 6144); g1 = ld_sc1_64(rs, o1); u1 = ld_sc1_64(rs, o1 + 6144); hv = ld_sc1_64(rs, oh);
        const bool ok = (g0[1] == tag) & (u0[1] == tag) & (g1[1] == tag) & (u1[1] == tag) & (hv[1] == tag);
        if (__all(ok)) break;
        __builtin_amdgcn_s_sleep(PSLEEP);
      }
    }
    STAMP(11);
    auto glu = [](float g, float u) -> float { return rbf(__builtin_amdgcn_rcpf(1.0f + __builtin_amdgcn_exp2f(-g * 1.4426950408889634f))) * u; };
    const u32 ha = pk_bf16(glu(lo_f(g0[0]), lo_f(u0[0])), glu(hi_f(g0[0]), hi_f(u0[0])));
    const u32 hb = pk_bf16(glu(lo_f(g1[0]), lo_f(u1[0])), glu(hi_f(g1[0]), hi_f(u1[0])));
    const u32 hh0 = dppu<0xA0>(ha), hh1 = dppu<0xF5>(ha);
    float ac[8];
#pragma unroll
    for (int c = 0; c < 8; ++c) ac[c] = dot2(dw[2][c], hb, 0.f);
#pragma unroll
    for (int c = 0; c < 8; ++c) ac[c] = dot2(dw[0][c], hh0, ac[c]);
#pragma unroll
    for (int c = 0; c < 8; ++c) ac[c] = dot2(dw[1][c], hh1, ac[c]);
    float b4[4], b2[2];
#pragma unroll
    for (int c = 0; c < 4; ++c) b4[c] = tr32(ac[c], ac[c + 4]);
#pragma unroll
    for (int c = 0; c < 2; ++c) b2[c] = tr16(b4[c], b4[c + 2]);
#pragma unroll
    for (int c = 0; c < 2; ++c) { b2[c] += dpp<ROR8>(b2[c]); b2[c] += dpp<ROR4>(b2[c]); b2[c] += dpp<XOR2>(b2[c]); }
    if ((lane & 14) == 0) {
      const int col = ch * 8 + (lane >> 5) * 4 + ((lane >> 4) & 1) * 2;
      *(LAS f32x2*)(L + L_T + tw * 16 + col) = f32x2{b2[0], b2[1]};
    }
    WG_BARRIER();
    if (tw == 0) {
      f32x2 t = *(const LAS f32x2*)(L + L_T + r8 * 16 + c8 * 2);
      t[0] += dpp<ROR8>(t[0]); t[1] += dpp<ROR8>(t[1]);
      t[0] = xrow_sum(t[0]); t[1] = xrow_sum(t[1]);
      const u32 dn = pk_bf16(t[0], t[1]);
      if (lane < 8) ((GAS u32*)out_)[dc * 8 + lane] = pk_bf16(lo_f(hv[0]) + lo_f(dn), hi_f(hv[0]) + hi_f(dn));
      if (dc == 0 && lane == 0) __hip_atomic_store((GAS u32*)(wsb + WS_EPOCH), tag, RLX_AGENT);
    }
    STAMP(12);
    STAMP_OUT();
  }
}

extern "C" void kernel_launch(void* const* d_in, const int* in_sizes, int n_in,
                              void* d_out, int out_size, void* d_ws, size_t ws_size,
                              hipStream_t stream) {
  hipLaunchKernelGGL(fused_block, dim3(256), dim3(NTHR), 0, stream,
                     (const u16*)d_in[0], (const u16*)d_in[1], (const u16*)d_in[2], (const u16*)d_in[3], (const u16*)d_in[4],
                     (const u16*)d_in[6], (const u16*)d_in[7], (const u16*)d_in[8], (u16*)d_out, (unsigned char*)d_ws);
}
```

```cpp
#include <hip/hip_runtime.h>
#include <hip/hip_bf16.h>
#include <stdint.h>

#define STAMPS 0

typedef unsigned short u16;
typedef unsigned u32;
typedef unsigned long long u64;
typedef u32 u32x4 __attribute__((ext_vector_type(4)));
typedef u32 u32x2 __attribute__((ext_vector_type(2)));
typedef float f32x4 __attribute__((ext_vector_type(4)));
typedef float f32x2 __attribute__((ext_vector_type(2)));
typedef __bf16 bf16x8 __attribute__((ext_vector_type(8)));
typedef __bf16 bf16x2 __attribute__((ext_vector_type(2)));
#define GAS __attribute__((address_space(1)))
#define LAS __attribute__((address_space(3)))

constexpr int D = 640, H = 32, DH = 192, SK = 8192, W = 2048, F = 1536, HD = H * DH;
constexpr int NTHR = 768;
constexpr float LOG2E = 1.4426950408889634f;

constexpr int WIN = 8;
constexpr int KGATE = 24;
constexpr int WOWIN = 3;
constexpr int F_EARLY = 4;
constexpr int TG_QP = 395, TG_Q = 495, TG_WO = 750, TG_F = 760, TG_D = 870, TG_P = 920, TG_O = 990, TG_R = 1130;
constexpr int PF_BYTES = 16128, PF_REP = 3, PF_PER = 400;

constexpr int WS_EPOCH = 0;
constexpr int HS_REP = 2560;
constexpr int WS_HS = 4096;
constexpr int GU_REP = 12288;
constexpr int WS_GU = 32768;
constexpr int WS_YP = WS_GU + 4 * GU_REP;
constexpr int WS_QP = WS_YP + H * D * 4;
constexpr int OP_REC = 1024;
constexpr int WS_OP = WS_QP + H * 8 * DH * 4;
constexpr int WS_END = WS_OP + H * 8 * OP_REC;
constexpr int WS_STAMP = 2 << 20;
constexpr u32 SPIN_CAP = 1u << 18;

constexpr int C_Q = 0, C_KISS = 1, C_VISS = 2, C_O = 3, C_P = 4, C_PUB = 5, C_PF = 8;
constexpr int L_Q = 16;
constexpr int L_P = 112;
constexpr int L_ML = L_P + 384;
constexpr int L_O = L_ML + 8;
constexpr int L_T = L_O + 8 * 192;
constexpr int L_ST = L_T + 2048;
constexpr int L_X = L_ST + 16;
constexpr int L_END = L_X + 8 * 288;

__device__ __forceinline__ u32 fu(float f) { return __float_as_uint(f); }
__device__ __forceinline__ float uf(u32 u) { return __uint_as_float(u); }
__device__ __forceinline__ float lo_f(u32 u) { return uf(u << 16); }
__device__ __forceinline__ float hi_f(u32 u) { return uf(u & 0xffff0000u); }
__device__ __forceinline__ u32 pk_bf16(float lo, float hi) {
  bf16x2 v = {(__bf16)lo, (__bf16)hi};
  return __builtin_bit_cast(u32, v);
}
__device__ __forceinline__ float rbf(float f) { return (float)(__bf16)f; }
__device__ __forceinline__ float dot2(u32 a, u32 b, float acc) {
  return __builtin_amdgcn_fdot2_f32_bf16(__builtin_bit_cast(bf16x2, a), __builtin_bit_cast(bf16x2, b), acc, false);
}
__device__ __forceinline__ f32x4 mfma(u32x4 a, u32x4 b, f32x4 c) {
  return __builtin_amdgcn_mfma_f32_16x16x32_bf16(__builtin_bit_cast(bf16x8, a), __builtin_bit_cast(bf16x8, b), c, 0, 0, 0);
}
__device__ __forceinline__ u32x4 sel(u32 v, u32x4 m) { return u32x4{v & m[0], v & m[1], v & m[2], v & m[3]}; }
template <int CTRL> __device__ __forceinline__ float dpp(float x) {
  return __builtin_bit_cast(float, __builtin_amdgcn_mov_dpp(__builtin_bit_cast(int, x), CTRL, 0xf, 0xf, true));
}
template <int CTRL> __device__ __forceinline__ u32 dppu(u32 x) { return (u32)__builtin_amdgcn_mov_dpp((int)x, CTRL, 0xf, 0xf, true); }
constexpr int XOR1 = 0xB1, XOR2 = 0x4E, HMIR = 0x141, ROR8 = 0x128;
__device__ __forceinline__ float sum8(float x) { x += dpp<XOR1>(x); x += dpp<XOR2>(x); x += dpp<HMIR>(x); return x; }
__device__ __forceinline__ float max8(float x) {
  float r;
  asm volatile("s_nop 1\n\tv_max_f32_dpp %0, %1, %1 quad_perm:[1,0,3,2] row_mask:0xf bank_mask:0xf\n\t"
               "s_nop 1\n\tv_max_f32_dpp %0, %0, %0 quad_perm:[2,3,0,1] row_mask:0xf bank_mask:0xf\n\t"
               "s_nop 1\n\tv_max_f32_dpp %0, %0, %0 row_half_mirror row_mask:0xf bank_mask:0xf" : "=&v"(r) : "v"(x));
  return r;
}
__device__ __forceinline__ float max16(float x) {
  float r;
  asm volatile("s_nop 1\n\tv_max_f32_dpp %0, %1, %1 quad_perm:[1,0,3,2] row_mask:0xf bank_mask:0xf\n\t"
               "s_nop 1\n\tv_max_f32_dpp %0, %0, %0 quad_perm:[2,3,0,1] row_mask:0xf bank_mask:0xf\n\t"
               "s_nop 1\n\tv_max_f32_dpp %0, %0, %0 row_half_mirror row_mask:0xf bank_mask:0xf\n\t"
               "s_nop 1\n\tv_max_f32_dpp %0, %0, %0 row_ror:8 row_mask:0xf bank_mask:0xf\n\ts_nop 1" : "=&v"(r) : "v"(x));
  return r;
}
__device__ __forceinline__ float tr32(float a, float b) {
  auto t = __builtin_amdgcn_permlane32_swap(fu(a), fu(b), false, false);
  return uf(t[0]) + uf(t[1]);
}
__device__ __forceinline__ float tr16(float a, float b) {
  auto t = __builtin_amdgcn_permlane16_swap(fu(a), fu(b), false, false);
  return uf(t[0]) + uf(t[1]);
}
__device__ __forceinline__ float xrow_sum(float x) { x = tr16(x, x); return tr32(x, x); }
__device__ __forceinline__ float xrow_max(float x) {
  auto s = __builtin_amdgcn_permlane16_swap(fu(x), fu(x), false, false);
  x = fmaxf(uf(s[0]), uf(s[1]));
  auto t = __builtin_amdgcn_permlane32_swap(fu(x), fu(x), false, false);
  return fmaxf(uf(t[0]), uf(t[1]));
}
__device__ __forceinline__ float sum64(float x) { x = sum8(x); x += dpp<ROR8>(x); return xrow_sum(x); }
__device__ __forceinline__ f32x4 sum_r8(f32x4 t) {
  float w = tr16(tr32(t[0], t[1]), tr32(t[2], t[3]));
  w += dpp<ROR8>(w);
  auto sa = __builtin_amdgcn_permlane16_swap(fu(w), fu(w), false, false);
  auto se = __builtin_amdgcn_permlane32_swap(sa[0], sa[0], false, false);
  auto so = __builtin_amdgcn_permlane32_swap(sa[1], sa[1], false, false);
  return f32x4{uf(se[0]), uf(se[1]), uf(so[0]), uf(so[1])};
}

typedef __amdgpu_buffer_rsrc_t rsrc_t;
__device__ __forceinline__ rsrc_t mkrs(const void* p, int bytes) { return __builtin_amdgcn_make_buffer_rsrc((void*)p, 0, bytes, 0x00020000); }
__device__ __forceinline__ u32x4 ld_nt(rsrc_t rs, int off) { return __builtin_amdgcn_raw_buffer_load_b128(rs, off, 0, 2); }
__device__ __forceinline__ u32x4 ld_sc1(rsrc_t rs, int off) { return __builtin_amdgcn_raw_buffer_load_b128(rs, off, 0, 16); }
__device__ __forceinline__ u32x2 ld_sc1_64(rsrc_t rs, int off) { return __builtin_amdgcn_raw_buffer_load_b64(rs, off, 0, 16); }
__device__ __forceinline__ void st_sc1(rsrc_t rs, int off, u32x4 v) { __builtin_amdgcn_raw_buffer_store_b128(v, rs, off, 0, 16); }
template <int N> __device__ __forceinline__ void vmw() { asm volatile("s_waitcnt vmcnt(%0)" :: "i"(N) : "memory"); }

__device__ __forceinline__ u32x4 pack4(f32x4 v, u32 tag) {
  const u32 a = fu(v[0]) + 0x80u, b = fu(v[1]) + 0x80u, c = fu(v[2]) + 0x80u, d = fu(v[3]) + 0x80u;
  return u32x4{__builtin_amdgcn_perm(a, b, 0x07060503u), __builtin_amdgcn_perm(b, tag, 0x06050100u),
               __builtin_amdgcn_perm(c, d, 0x07060503u), __builtin_amdgcn_perm(d, tag, 0x06050100u)};
}
__device__ __forceinline__ f32x4 unpack4(u32x4 w) {
  return f32x4{uf(w[0] & 0xffffff00u), uf(__builtin_amdgcn_perm(w[0], w[1], 0x0403020cu)),
               uf(w[2] & 0xffffff00u), uf(__builtin_amdgcn_perm(w[2], w[3], 0x0403020cu))};
}
__device__ __forceinline__ bool ok4(u32x4 w, u32 tag) { return (((w[1] ^ tag) | (w[3] ^ tag)) & 0xffffu) == 0u; }

#define CBAR() asm volatile("" ::: "memory")
#define PIN(x) asm volatile("" : "+v"(x))
#define LGKM0() asm volatile("s_waitcnt lgkmcnt(0)" ::: "memory")
#define WG_BARRIER() asm volatile("s_waitcnt lgkmcnt(0)\n\ts_barrier" ::: "memory")
#define SCHED() __builtin_amdgcn_sched_barrier(0)

__device__ __forceinline__ void lds_wait_ge(volatile LAS u32* w, u32 n) {
#pragma nounroll
  for (u32 sp = 0; sp < SPIN_CAP; ++sp) {
    if (*w >= n) break;
    __builtin_amdgcn_s_sleep(1);
  }
  CBAR();
}
__device__ __forceinline__ void wait_until(u64 t) {
#pragma nounroll
  for (u32 sp = 0; sp < 4096; ++sp) {
    if (__builtin_amdgcn_s_memrealtime() >= t) break;
    __builtin_amdgcn_s_sleep(1);
  }
  CBAR();
}
__device__ __forceinline__ void lds_inc(LAS u32* w, int lane) {
  CBAR();
  if (lane == 0) __hip_atomic_fetch_add(w, 1u, __ATOMIC_RELAXED, __HIP_MEMORY_SCOPE_WORKGROUP);
  CBAR();
}

#define STAMP(i) do { } while (0)
#define STAMP_DECL do { } while (0)
#define STAMP_INIT() do { } while (0)
#define STAMP_OUT() do { } while (0)
#define L_TOTAL L_END

__global__ void __launch_bounds__(NTHR, 3)
fused_block(const u16* __restrict__ x_, const u16* __restrict__ kc_, const u16* __restrict__ vc_, const u16* __restrict__ wq_,
            const u16* __restrict__ wo_, const u16* __restrict__ wg_, const u16* __restrict__ wu_, const u16* __restrict__ wd_,
            u16* __restrict__ out_, unsigned char* ws_) {
  u64 pc0; asm volatile("s_getpc_b64 %0" : "=s"(pc0));
  STAMP_DECL;
  asm volatile("" :: "s"(x_), "s"(kc_), "s"(vc_), "s"(wq_), "s"(wo_), "s"(wg_), "s"(wu_), "s"(wd_), "s"(out_), "s"(ws_));
  u32 epw;
  asm volatile("s_load_dword %0, %1, 0x0" : "=s"(epw) : "s"(ws_) : "memory");
  __shared__ __attribute__((aligned(16))) u32 smem[L_TOTAL];
  LAS u32* L = (LAS u32*)smem;
  volatile LAS u32* LV = (volatile LAS u32*)smem;
  const int tid = threadIdx.x, lane = tid & 63, wave = __builtin_amdgcn_readfirstlane(tid >> 6);
  const int b = blockIdx.x;
  const int xcd = b & 7, idx = b >> 3;
  const int h = xcd * 4 + (idx >> 3), s = idx & 7;
  const rsrc_t rs = mkrs(ws_, WS_END);
  const int r8 = lane >> 3, c8 = lane & 7;
  const u64 tstart = __builtin_amdgcn_s_memrealtime();
  u32x4 msk;
#pragma unroll
  for (int d = 0; d < 4; ++d) msk[d] = (d == (c8 >> 1)) ? ((c8 & 1) ? 0xffff0000u : 0x0000ffffu) : 0u;
  u32 tag;
#define FIRST_BARRIER() do { asm volatile("s_waitcnt lgkmcnt(0)\n\ts_barrier\n\ts_add_u32 %0, %1, 1" : "=s"(tag) : "s"(epw) : "memory", "scc"); STAMP_INIT(); } while (0)
  const int g0 = 36 * s + (s > 3 ? s - 3 : 0), ng = s >= 3 ? 37 : 36;
  const size_t kvoff = ((size_t)h * SK + (SK - W) + g0 * 8) * DH;

  if (wave < 8 && s == 7) {
    if (wave == 4 && idx >= 16) {
      const rsrc_t rc = mkrs((const void*)(pc0 & ~127ull), PF_BYTES);
      const int off = (((idx >> 3) - 2) * 64 + lane) * 128;
      u32 t0 = __builtin_amdgcn_raw_buffer_load_b32(rc, off, 0, 2);
      FIRST_BARRIER();
      asm volatile("" :: "v"(t0));
#pragma nounroll
      for (int r = 1; r < PF_REP; ++r) {
        wait_until(tstart + r * PF_PER);
        u32 t1 = __builtin_amdgcn_raw_buffer_load_b32(rc, off, 0, 2);
        asm volatile("" :: "v"(t1));
      }
    } else {
      if (wave == 5) {
        const size_t win = ((size_t)h * SK + (SK - W)) * DH * 2;
        const int q4 = idx >> 3;
        const GAS char* p;
        if (lane < 13) p = (const GAS char*)vc_ + win + (lane < 12 ? lane * 65536 : W * DH * 2 - 64);
        else if (lane < 26) p = (const GAS char*)kc_ + win + (lane < 25 ? (lane - 13) * 65536 : W * DH * 2 - 64);
        else if (lane < 31) p = (const GAS char*)wo_ + (size_t)h * DH * D * 2 + (lane < 30 ? (lane - 26) * 65536 : DH * D * 2 - 64);
        else if (lane < 39) p = (const GAS char*)wg_ + q4 * 491520 + (lane - 31) * 61440;
        else if (lane < 47) p = (const GAS char*)wu_ + q4 * 491520 + (lane - 39) * 61440;
        else if (lane < 55) p = (const GAS char*)wd_ + q4 * 491520 + (lane - 47) * 61440;
        else p = (const GAS char*)out_;
        u32 t0 = __builtin_nontemporal_load((const GAS u32*)p);
        FIRST_BARRIER();
        asm volatile("" :: "v"(t0));
      } else {
        FIRST_BARRIER();
      }
    }
  } else if (wave < 4) {
    __builtin_amdgcn_s_setprio(2);
    const rsrc_t rk = mkrs(kc_ + kvoff, ng * 3072);
    const int loff = wave * 3072 + r8 * 384 + c8 * 16;
    u32x4 kr[30];
#pragma unroll
    for (int i = 0; i < 30; ++i) {
      if (i == WIN) FIRST_BARRIER();
      if (i >= WIN) vmw<WIN - 1>();
      if (i == KGATE) lds_inc(L + C_KISS, lane);
      kr[i] = ld_nt(rk, loff + (i / 3) * 12288 + (i % 3) * 128);
      SCHED();
    }
    float dd[10];
    const bool sbok = c8 == 0 || (c8 == 1 && 36 + wave < ng);
    int padr = L_P + wave * 96 + r8 * 12 + c8, padr2 = c8 < 2 ? padr + 8 : L_T + lane;
    PIN(padr); PIN(padr2);
#pragma unroll
    for (int g = 0; g < 10; ++g) { dd[g] = 0.f; PIN(dd[g]); }
    STAMP(1);
    lds_wait_ge(LV + C_Q, 3);
    STAMP(2);
    u32x4 qp[3];
#pragma unroll
    for (int t = 0; t < 3; ++t) qp[t] = *(const LAS u32x4*)(L + L_Q + t * 32 + c8 * 4);
#pragma unroll
    for (int e = 0; e < 12; ++e) {
#pragma unroll
      for (int g = 0; g < 10; ++g) dd[g] = dot2(kr[g * 3 + e / 4][e % 4], qp[e / 4][e % 4], dd[g]);
      SCHED();
    }
    const bool b0 = c8 & 1, b1 = c8 & 2, b2 = c8 & 4;
    float e4[4], e2[2];
#pragma unroll
    for (int i = 0; i < 4; ++i) { const float keep = b2 ? dd[i + 4] : dd[i], give = b2 ? dd[i] : dd[i + 4]; e4[i] = keep + dpp<HMIR>(give); }
#pragma unroll
    for (int i = 0; i < 2; ++i) { const float keep = b1 ? e4[i + 2] : e4[i], give = b1 ? e4[i] : e4[i + 2]; e2[i] = keep + dpp<XOR2>(give); }
    float sa = (b0 ? e2[1] : e2[0]) + dpp<XOR1>(b0 ? e2[0] : e2[1]);
    float sb = (b0 ? dd[9] : dd[8]) + dpp<XOR1>(b0 ? dd[8] : dd[9]);
    sb += dpp<XOR2>(sb);
    sb += dpp<0x1B>(dpp<HMIR>(sb));
    sa = rbf(rbf(sa) * 0.07216878364870323f);
    sb = rbf(rbf(sb) * 0.07216878364870323f);
    sb = sbok ? sb : -INFINITY;
    const float m = xrow_max(max16(fmaxf(sa, sb)));
    const float pa = __builtin_amdgcn_exp2f((sa - m) * LOG2E), pb = __builtin_amdgcn_exp2f((sb - m) * LOG2E);
    L[padr] = pk_bf16(pa, pa);
    L[padr2] = pk_bf16(pb, pb);
    CBAR();
    if (lane == 0) LV[C_PF + wave] = 1u;
    const float l = sum64(pa + pb);
    if (lane == 0) { L[L_ML + wave] = fu(m); L[L_ML + 4 + wave] = fu(l); }
    lds_inc(L + C_P, lane);
  } else if (wave < 8) {
    __builtin_amdgcn_s_setprio(1);
    const int w = wave - 4;
    const rsrc_t rv = mkrs(vc_ + kvoff, ng * 3072);
    const int loff = w * 3072 + r8 * 384 + c8 * 16;
    FIRST_BARRIER();
    lds_wait_ge(LV + C_KISS, 4);
    STAMP(1);
    u32x4 vr[30];
#pragma unroll
    for (int i = 0; i < 30; ++i) {
      const int j = (i + 27) % 30;
      if (i >= WIN) vmw<WIN - 1>();
      if (i == 29) lds_inc(L + C_VISS, lane);
      vr[j] = ld_nt(rv, loff + (j / 3) * 12288 + (j % 3) * 128);
      SCHED();
    }
    STAMP(2);
    int wadr = ((lane >> 5) == ((lane >> 3) & 1) ? L_O + (w * 2 + (lane >> 5)) * 192 : L_T + 64 + w * 192) + c8 * 8 + ((lane >> 4) & 1) * 4;
    int radr = L_O + 24 * r8 + 4 * (c8 < 6 ? c8 : 0);
    int sadr = WS_OP + (h * 8 + s) * OP_REC + lane * 16;
    u32 tadr = (u32)(uintptr_t)(L + C_O), one = 1u;
    PIN(wadr); PIN(radr); PIN(sadr); PIN(tadr); PIN(one);
    lds_wait_ge(LV + C_PF + w, 1);
    STAMP(3);
    const LAS u32* pr = L + L_P + w * 96 + r8 * 12;
    const u32x4 p0 = *(const LAS u32x4*)(pr), p1 = *(const LAS u32x4*)(pr + 4);
    const u32x2 p2 = *(const LAS u32x2*)(pr + 8);
    float M, Ls, fw;
    f32x4 acc[2][3];
#pragma unroll
    for (int gg = 0; gg < 10; ++gg) {
      const int g = (gg + 9) % 10;
      if (gg == 5) {
        SCHED();
        lds_wait_ge(LV + C_P, 4);
        const f32x4 mm = *(const LAS f32x4*)(L + L_ML), ll = *(const LAS f32x4*)(L + L_ML + 4);
        M = fmaxf(fmaxf(mm[0], mm[1]), fmaxf(mm[2], mm[3]));
        f32x4 ff;
#pragma unroll
        for (int k = 0; k < 4; ++k) ff[k] = __builtin_amdgcn_exp2f((mm[k] - M) * LOG2E);
        Ls = (ff[0] * ll[0] + ff[1] * ll[1]) + (ff[2] * ll[2] + ff[3] * ll[3]);
        fw = w == 0 ? ff[0] : (w == 1 ? ff[1] : (w == 2 ? ff[2] : ff[3]));
        PIN(M); PIN(Ls); PIN(fw);
      }
      const u32x4 A = sel(g < 4 ? p0[g & 3] : (g < 8 ? p1[g & 3] : p2[g & 1]), msk);
#pragma unroll
      for (int t = 0; t < 3; ++t) {
        if (gg == 9) acc[0][t] = mfma(A, vr[g * 3 + t], acc[0][t] + acc[1][t]);
        else acc[gg & 1][t] = mfma(A, vr[g * 3 + t], gg < 2 ? f32x4{0.f, 0.f, 0.f, 0.f} : acc[gg & 1][t]);
      }
    }
#pragma unroll
    for (int t = 0; t < 3; ++t) *(LAS f32x4*)(L + wadr + t * 64) = acc[0][t] * fw;
    STAMP(4);
    u32 old;
    asm volatile("s_mov_b64 exec, 1\n\tds_add_rtn_u32 %0, %1, %2\n\ts_mov_b64 exec, -1\n\ts_waitcnt lgkmcnt(0)" : "=&v"(old) : "v"(tadr), "v"(one) : "memory");
    if (__builtin_amdgcn_readfirstlane(old) == 3) {
      f32x4 pt[8];
#pragma unroll
      for (int k = 0; k < 8; ++k) pt[k] = *(const LAS f32x4*)(L + radr + k * 192);
      f32x4 o = ((pt[0] + pt[1]) + (pt[2] + pt[3])) + ((pt[4] + pt[5]) + (pt[6] + pt[7]));
      if (c8 == 6) { o[0] = M; o[1] = Ls; }
      u32x4 pk = pack4(o, tag);
      PIN(pk);
      wait_until(tstart + TG_P);
      st_sc1(rs, sadr, pk);
      if (lane == 0) LV[C_PUB] = 1u;
      STAMP(5);
    }
    return;
  } else {
    const int a = wave - 8;
    __builtin_amdgcn_s_setprio(3);
    if (a < 3) {
      const GAS char* xb = (const GAS char*)x_;
      const u32x4 xa = *(const GAS u32x4*)(xb + lane * 16);
      const u32 xc = *(const GAS u32*)(xb + 1024 + lane * 4);
      const GAS u32* xr = (const GAS u32*)(xb + 160 * s + 20 * r8);
      u32 xo[5];
#pragma unroll
      for (int i = 0; i < 5; ++i) xo[i] = xr[i];
      const rsrc_t rq = mkrs(wq_ + (size_t)80 * s * HD + h * DH + a * 64, 79 * HD * 2 + 128);
      const int qoff = 10 * r8 * (HD * 2) + c8 * 16;
      u32x4 wr[10];
#pragma unroll
      for (int g = 0; g < 10; ++g) wr[g] = ld_nt(rq, qoff + g * (HD * 2));
      SCHED();
      FIRST_BARRIER();
      SCHED();
      const float xv[10] = {lo_f(xa[0]), hi_f(xa[0]), lo_f(xa[1]), hi_f(xa[1]), lo_f(xa[2]), hi_f(xa[2]), lo_f(xa[3]), hi_f(xa[3]), lo_f(xc), hi_f(xc)};
      float sm = 0.f;
#pragma unroll
      for (int i = 0; i < 10; ++i) sm += xv[i];
      const float mean = sum64(sm) * (1.0f / D);
      float sq = 0.f;
#pragma unroll
      for (int i = 0; i < 10; ++i) { const float dd = xv[i] - mean; sq += dd * dd; }
      const float var = rbf(sum64(sq) * (1.0f / D));
      const float mb = rbf(mean);
      const float rinv = 1.0f / rbf(__builtin_sqrtf(rbf(var + 1e-5f)));
      f32x4 acc = {0.f, 0.f, 0.f, 0.f};
#pragma unroll
      for (int g = 0; g < 10; ++g) {
        const float xg = (g & 1) ? hi_f(xo[g >> 1]) : lo_f(xo[g >> 1]);
        const float xn = rbf(xg - mb) * rinv;
        acc = mfma(sel(pk_bf16(xn, xn), msk), wr[g], acc);
      }
#pragma unroll
      for (int i = 0; i < 4; ++i) {
        const float up = dpp<ROR8>(acc[i]);
        auto t = __builtin_amdgcn_permlane32_swap(fu(up), fu(up), false, false);
        acc[i] += uf(t[1]);
      }
      u32x4 pk = pack4(acc, tag);
      PIN(pk);
      STAMP(1);
      wait_until(tstart + TG_QP);
      if (lane < 32 && (lane & 8) == 0) st_sc1(rs, WS_QP + (h * 8 + s) * (DH * 4) + a * 256 + (c8 + 8 * (lane >> 4)) * 16, pk);
      if (s < 7) {
        const int gb = WS_QP + (h * 8 + (lane >> 4)) * (DH * 4) + a * 256 + (lane & 15) * 16;
        u32x4 v[2];
        bool ok[2] = {false, false};
        wait_until(tstart + TG_Q);
#pragma nounroll
        for (u32 sp = 0; sp < SPIN_CAP; ++sp) {
          CBAR();
#pragma unroll
          for (int i = 0; i < 2; ++i) if (!ok[i]) v[i] = ld_sc1(rs, gb + i * (4 * DH * 4));
#pragma unroll
          for (int i = 0; i < 2; ++i) ok[i] = ok4(v[i], tag);
          if (__all(ok[0] & ok[1])) break;
          __builtin_amdgcn_s_sleep(2);
        }
        f32x4 q = unpack4(v[0]) + unpack4(v[1]);
#pragma unroll
        for (int i = 0; i < 4; ++i) q[i] = xrow_sum(q[i]);
        if (lane < 16) *(LAS u32x2*)(L + L_Q + a * 32 + 4 * c8 + 2 * (lane >> 3)) = u32x2{pk_bf16(q[0], q[1]), pk_bf16(q[2], q[3])};
        lds_inc(L + C_Q, lane);
        STAMP(2);
      }
    } else {
      if (lane < 16) L[lane] = 0u;
      FIRST_BARRIER();
    }
  }
  __builtin_amdgcn_s_setprio(0);

  if (s == 7) {
    const int j4 = idx >> 3;
    const int rI = j4 < 2 ? 2 * xcd + j4 : 14 + 2 * xcd + j4;
    if (rI < 20) {
      const int dt = rI;
      const int r16 = lane >> 2, c4 = lane & 3;
      u32 dw[4][8];
      {
        const rsrc_t rd = mkrs(wd_ + (size_t)128 * wave * D + dt * 32, 127 * D * 2 + 64);
        const int doff = 8 * r16 * (D * 2) + c4 * 16;
        wait_until(tstart + TG_D);
        u32x4 t[8];
#pragma unroll
        for (int i = 0; i < 8; ++i) { t[i] = ld_nt(rd, doff + i * (D * 2)); SCHED(); }
#pragma unroll
        for (int k = 0; k < 4; ++k)
#pragma unroll
          for (int d = 0; d < 4; ++d) {
            dw[k][2 * d] = __builtin_amdgcn_perm(t[2 * k + 1][d], t[2 * k][d], 0x05040100u);
            dw[k][2 * d + 1] = __builtin_amdgcn_perm(t[2 * k + 1][d], t[2 * k][d], 0x07060302u);
          }
#pragma unroll
        for (int k = 0; k < 4; ++k)
#pragma unroll
          for (int c = 0; c < 8; ++c) PIN(dw[k][c]);
      }
      float ac[8];
#pragma unroll
      for (int c = 0; c < 8; ++c) { ac[c] = 0.f; PIN(ac[c]); }
      int wadr = L_T + (wave * 4 + ((lane >> 2) & 3)) * 32 + c4 * 8 + (lane >> 5) * 4 + ((lane >> 4) & 1) * 2;
      int radr = L_T + r8 * 192 + c8 * 4;
      int oadr = dt * 64 + c8 * 8;
      PIN(wadr); PIN(radr); PIN(oadr);
      const rsrc_t ro = mkrs(out_, D * 2);
      STAMP(10);
      u32x2 gv, uv;
      u32x4 hv = {0u, tag, 0u, tag};
      {
        const int o0 = WS_GU + (rI & 3) * GU_REP + (64 * wave + lane) * 8;
        const int oh = WS_HS + xcd * HS_REP + (dt * 16 + c8 * 2) * 8;
#pragma nounroll
        for (u32 sp = 0; sp < SPIN_CAP; ++sp) {
          CBAR();
          gv = ld_sc1_64(rs, o0); uv = ld_sc1_64(rs, o0 + 6144);
          if (wave == 0) hv = ld_sc1(rs, oh);
          if (__all(((gv[1] ^ tag) | (uv[1] ^ tag) | (hv[1] ^ tag) | (hv[3] ^ tag)) == 0u)) break;
        }
      }
      STAMP(11);
      auto glu = [](float g, float u) -> float { return rbf(__builtin_amdgcn_rcpf(1.0f + __builtin_amdgcn_exp2f(-g * LOG2E))) * u; };
      const u32 ha = pk_bf16(glu(lo_f(gv[0]), lo_f(uv[0])), glu(hi_f(gv[0]), hi_f(uv[0])));
      const u32 hh[4] = {dppu<0x00>(ha), dppu<0x55>(ha), dppu<0xAA>(ha), dppu<0xFF>(ha)};
#pragma unroll
      for (int k = 0; k < 4; ++k)
#pragma unroll
        for (int c = 0; c < 8; ++c) ac[c] = dot2(dw[k][c], hh[k], ac[c]);
      float b4[4];
#pragma unroll
      for (int c = 0; c < 4; ++c) b4[c] = tr32(ac[c], ac[c + 4]);
      *(LAS f32x2*)(L + wadr) = f32x2{tr16(b4[0], b4[2]), tr16(b4[1], b4[3])};
      WG_BARRIER();
      if (wave == 0) {
        f32x4 p[6];
#pragma unroll
        for (int k = 0; k < 6; ++k) p[k] = *(const LAS f32x4*)(L + radr + k * 32);
        const f32x4 t = sum_r8(((p[0] + p[1]) + (p[2] + p[3])) + (p[4] + p[5]));
        const u32 d0 = pk_bf16(t[0], t[1]), d1 = pk_bf16(t[2], t[3]);
        const u32x2 ov = {pk_bf16(lo_f(hv[0]) + lo_f(d0), hi_f(hv[0]) + hi_f(d0)), pk_bf16(lo_f(hv[2]) + lo_f(d1), hi_f(hv[2]) + hi_f(d1))};
        if (lane < 8) __builtin_amdgcn_raw_buffer_store_b64(ov, ro, oadr, 0, 16);
        if (rI == 0 && lane == 0) __builtin_amdgcn_raw_buffer_store_b32(tag, rs, WS_EPOCH, 0, 16);
      }
      STAMP(12);
    } else if (rI < 30 && wave < 2) {
      const int ru = (rI - 20) * 2 + wave;
      const u32x2 xp = *(const GAS u32x2*)((const GAS char*)x_ + (ru * 32 + c8 * 4) * 2);
      f32x4 xf = {lo_f(xp[0]), hi_f(xp[0]), lo_f(xp[1]), hi_f(xp[1])};
      int sadr = WS_HS + r8 * HS_REP + (ru * 16 + c8 * 2) * 8;
      PIN(xf); PIN(sadr);
      const int gb = WS_YP + (r8 * D + ru * 32 + c8 * 4) * 4;
      u32x4 v[4];
      bool ok[4] = {false, false, false, false};
      wait_until(tstart + TG_R);
      STAMP(13);
#pragma nounroll
      for (u32 sp = 0; sp < SPIN_CAP; ++sp) {
        CBAR();
#pragma unroll
        for (int i = 0; i < 4; ++i) if (!ok[i]) v[i] = ld_sc1(rs, gb + i * (8 * D * 4));
#pragma unroll
        for (int i = 0; i < 4; ++i) ok[i] = ok4(v[i], tag);
        if (__all(ok[0] & ok[1] & ok[2] & ok[3])) break;
      }
      STAMP(14);
      const f32x4 y = sum_r8((unpack4(v[0]) + unpack4(v[1])) + (unpack4(v[2]) + unpack4(v[3])));
      const u32 a0 = pk_bf16(y[0], y[1]), a1 = pk_bf16(y[2], y[3]);
      st_sc1(rs, sadr, u32x4{pk_bf16(xf[0] + lo_f(a0), xf[1] + hi_f(a0)), tag, pk_bf16(xf[2] + lo_f(a1), xf[3] + hi_f(a1)), tag});
      STAMP(15);
    }
    STAMP_OUT();
    return;
  }

  const int tw = wave < 4 ? wave : wave - 4;
  if (s < 5) {
    lds_wait_ge(LV + C_VISS, 4);
    STAMP(3);
    const int g4 = lane >> 4, z = lane & 15;
    const rsrc_t ro = mkrs(wo_ + (size_t)(h * DH + 24 * tw) * D + s * 128, 23 * D * 2 + 256);
    const int ooff = 6 * g4 * (D * 2) + z * 16;
    u32x4 wr[6];
    wait_until(tstart + TG_WO);
#pragma unroll
    for (int k = 0; k < 6; ++k) { if (k >= WOWIN) vmw<WOWIN - 1>(); wr[k] = ld_nt(ro, ooff + k * (D * 2)); SCHED(); }
    u32x4 mA, mB;
#pragma unroll
    for (int d = 0; d < 4; ++d) { mA[d] = z < 8 ? msk[d] : 0u; mB[d] = z < 8 ? 0u : msk[d]; }
    int xw = L_X + tw * 288 + r8 * 36 + c8 * 4, xm = L_X + tw * 288 + c8 * 36 + 24, xo = L_X + tw * 288 + c8 * 36 + 3 * r8;
    int wadr = L_T + (tw * 2 + (g4 >> 1)) * 128 + z * 8 + (g4 & 1) * 4;
    int radr = L_T + r8 * 256 + tw * 32 + c8 * 4;
    int sadr = WS_YP + (h * D + s * 128 + tw * 32 + c8 * 4) * 4;
    PIN(mA); PIN(mB); PIN(xw); PIN(xm); PIN(xo); PIN(wadr); PIN(radr); PIN(sadr);
    lds_wait_ge(LV + C_PUB, 1);
    STAMP(4);
    const int gb = WS_OP + (h * 8 + r8) * OP_REC + tw * 128 + c8 * 16;
    u32x4 va = {c8 == 6 ? 0xff800000u : 0u, tag & 0xffffu, 0u, tag & 0xffffu};
    bool ok = r8 == 7;
    wait_until(tstart + TG_O);
#pragma nounroll
    for (u32 sp = 0; sp < SPIN_CAP; ++sp) {
      CBAR();
      if (!ok) va = ld_sc1(rs, gb);
      ok = ok4(va, tag);
      if (__all(ok)) break;
    }
    STAMP(5);
    *(LAS f32x4*)(L + xw) = unpack4(va);
    CBAR();
    const f32x2 ml = *(const LAS f32x2*)(L + xm);
    const float o0 = uf(L[xo]), o1 = uf(L[xo + 1]), o2 = uf(L[xo + 2]);
    SCHED();
    const float f = __builtin_amdgcn_exp2f((ml[0] - max8(ml[0])) * LOG2E);
    float t0 = f * o0, t1 = f * o1, t2 = f * o2, t3 = f * ml[1];
    t0 += dpp<XOR1>(t0); t1 += dpp<XOR1>(t1); t2 += dpp<XOR1>(t2); t3 += dpp<XOR1>(t3);
    t3 += dpp<XOR2>(t3); t0 += dpp<XOR2>(t0); t1 += dpp<XOR2>(t1); t2 += dpp<XOR2>(t2);
    t3 += dpp<HMIR>(t3); t0 += dpp<HMIR>(t0); t1 += dpp<HMIR>(t1); t2 += dpp<HMIR>(t2);
    const float li = __builtin_amdgcn_rcpf(t3);
    t0 *= li; t1 *= li; t2 *= li;
    const u32 v0 = pk_bf16(t0, t0), v1 = pk_bf16(t1, t1), v2 = pk_bf16(t2, t2);
    const f32x4 zero = {0.f, 0.f, 0.f, 0.f};
    f32x4 aA = mfma(sel(v0, mA), wr[0], zero), aB = mfma(sel(v0, mB), wr[3], zero);
    aA = mfma(sel(v1, mA), wr[1], aA); aB = mfma(sel(v1, mB), wr[4], aB);
    aA = mfma(sel(v2, mA), wr[2], aA);
    aA = mfma(sel(v2, mB), wr[5], aA);
    *(LAS f32x4*)(L + wadr) = aA + aB;
    WG_BARRIER();
    if (tw < 4) {
      const f32x4 t = sum_r8(*(const LAS f32x4*)(L + radr) + *(const LAS f32x4*)(L + radr + 128));
      const u32x4 pk = pack4(t, tag);
      if (lane < 8) st_sc1(rs, sadr, pk);
    }
    STAMP(6);
    STAMP_OUT();
    return;
  }

  const int fi = (idx >> 3) * 2 + (s - 5);
  if (fi < 6) {
    const int ft = xcd * 6 + fi, mat = ft & 1, tile = ft >> 1;
    lds_wait_ge(LV + C_VISS, 4);
    STAMP(3);
    u32 gw[5][8];
    {
      const rsrc_t rg = mkrs((mat ? wu_ : wg_) + (size_t)80 * tw * F + tile * 64, 79 * F * 2 + 128);
      const int goff = 10 * r8 * (F * 2) + c8 * 16;
      u32x4 t[10];
      wait_until(tstart + TG_F);
#pragma unroll
      for (int i = 0; i < 10; ++i) {
        if (i == F_EARLY) { SCHED(); lds_wait_ge(LV + C_PUB, 1); SCHED(); }
        t[i] = ld_nt(rg, goff + i * (F * 2));
        SCHED();
      }
      SCHED();
#pragma unroll
      for (int i = 0; i < 5; ++i)
#pragma unroll
        for (int d = 0; d < 4; ++d) {
          gw[i][2 * d] = __builtin_amdgcn_perm(t[2 * i + 1][d], t[2 * i][d], 0x05040100u);
          gw[i][2 * d + 1] = __builtin_amdgcn_perm(t[2 * i + 1][d], t[2 * i][d], 0x07060302u);
        }
#pragma unroll
      for (int i = 0; i < 5; ++i)
#pragma unroll
        for (int c = 0; c < 8; ++c) PIN(gw[i][c]);
    }
    int wadr = L_T + (tw * 2 + ((lane >> 3) & 1)) * 64 + c8 * 8 + (lane >> 5) * 4 + ((lane >> 4) & 1) * 2;
    int radr = L_T + (lane >> 4) * 256 + (lane & 15) * 4;
    auto gemv = [&](const u32 (&hn)[5], float (&ac)[8]) {
#pragma unroll
      for (int i = 0; i < 5; ++i)
#pragma unroll
        for (int c = 0; c < 8; ++c) ac[c] = dot2(gw[i][c], hn[i], ac[c]);
      float b4[4];
#pragma unroll
      for (int c = 0; c < 4; ++c) b4[c] = tr32(ac[c], ac[c + 4]);
      *(LAS f32x2*)(L + wadr) = f32x2{tr16(b4[0], b4[2]), tr16(b4[1], b4[3])};
    };
    auto total = [&]() -> float {
      const f32x4 g = (*(const LAS f32x4*)(L + radr) + *(const LAS f32x4*)(L + radr + 64)) + (*(const LAS f32x4*)(L + radr + 128) + *(const LAS f32x4*)(L + radr + 192));
      return tr16(tr32(g[0], g[2]), tr32(g[1], g[3]));
    };
    float ac[8];
    {
      const u32 ones[5] = {0x3f803f80u, 0x3f803f80u, 0x3f803f80u, 0x3f803f80u, 0x3f803f80u};
#pragma unroll
      for (int c = 0; c < 8; ++c) ac[c] = 0.f;
      gemv(ones, ac);
    }
    WG_BARRIER();
    float cw = 0.f;
    if (tw == 0) cw = total();
    PIN(cw);
    WG_BARRIER();
#pragma unroll
    for (int c = 0; c < 8; ++c) { ac[c] = 0.f; PIN(ac[c]); }
    float e5 = c8 < 5 ? 1.0f : 0.0f;
    int sadr = WS_GU + (lane >> 4) * GU_REP + mat * 6144 + tile * 256 + (lane & 15) * 16;
    int stw = L_ST + tw, str = L_ST + c8;
    PIN(e5); PIN(sadr); PIN(wadr); PIN(radr); PIN(stw); PIN(str);
    STAMP(7);
    u32x2 hv;
    {
      const int o0 = WS_HS + xcd * HS_REP + (40 * tw + 5 * r8 + (c8 < 5 ? c8 : 0)) * 8;
#pragma nounroll
      for (u32 sp = 0; sp < SPIN_CAP; ++sp) {
        CBAR();
        hv = ld_sc1_64(rs, o0);
        if (__all(hv[1] == tag)) break;
      }
    }
    STAMP(8);
    u32 hn[5];
    hn[0] = __builtin_amdgcn_ds_swizzle(hv[0], 0x18 | (0 << 5));
    hn[1] = __builtin_amdgcn_ds_swizzle(hv[0], 0x18 | (1 << 5));
    hn[2] = __builtin_amdgcn_ds_swizzle(hv[0], 0x18 | (2 << 5));
    hn[3] = __builtin_amdgcn_ds_swizzle(hv[0], 0x18 | (3 << 5));
    hn[4] = __builtin_amdgcn_ds_swizzle(hv[0], 0x18 | (4 << 5));
    const float pv = lo_f(__builtin_amdgcn_readfirstlane(hv[0]));
    const float d0 = (lo_f(hv[0]) - pv) * e5, d1 = (hi_f(hv[0]) - pv) * e5;
    float s1 = sum8(d0 + d1), s2 = sum8(d0 * d0 + d1 * d1);
    s1 += dpp<ROR8>(s1); s2 += dpp<ROR8>(s2);
    float ss = tr32(s1, s2);
    ss = tr16(ss, ss);
    gemv(hn, ac);
    {
      auto sw = __builtin_amdgcn_permlane32_swap(fu(ss), fu(ss), false, false);
      const float t1 = uf(sw[0]), t2 = uf(sw[1]);
      if (lane == 0) { L[stw] = fu(pv + t1 * (1.0f / 80.0f)); L[stw + 8] = fu(t2 - t1 * t1 * (1.0f / 80.0f)); }
    }
    WG_BARRIER();
    if (tw == 0) {
      const float mw = uf(L[str]), qw = uf(L[str + 8]);
      const float G = total();
      const float mean = sum8(mw) * 0.125f;
      const float var = rbf(sum8(qw + 80.0f * (mw - mean) * (mw - mean)) * (1.0f / D));
      const float rinv = __builtin_amdgcn_rcpf(rbf(__builtin_amdgcn_sqrtf(rbf(var + 1e-5f))));
      const float g = (G - rbf(mean) * cw) * rinv;
      auto sa = __builtin_amdgcn_permlane16_swap(fu(g), fu(g), false, false);
      const u32 pr = pk_bf16(uf(sa[0]), uf(sa[1]));
      auto sb = __builtin_amdgcn_permlane32_swap(pr, pr, false, false);
      st_sc1(rs, sadr, u32x4{sb[0], tag, sb[1], tag});
    }
    STAMP(9);
  }
  STAMP_OUT();
}

extern "C" void kernel_launch(void* const* d_in, const int* in_sizes, int n_in,
                              void* d_out, int out_size, void* d_ws, size_t ws_size,
                              hipStream_t stream) {
  hipLaunchKernelGGL(fused_block, dim3(256), dim3(NTHR), 0, stream,
                     (const u16*)d_in[0], (const u16*)d_in[1], (const u16*)d_in[2], (const u16*)d_in[3], (const u16*)d_in[4],
                     (const u16*)d_in[6], (const u16*)d_in[7], (const u16*)d_in[8], (u16*)d_out, (unsigned char*)d_ws);
}
```

```cpp
#include <hip/hip_runtime.h>
#include <hip/hip_bf16.h>
#include <stdint.h>

typedef unsigned short u16;
typedef unsigned u32;
typedef unsigned long long u64;
typedef u32 u32x4 __attribute__((ext_vector_type(4)));
typedef u32 u32x2 __attribute__((ext_vector_type(2)));
typedef float f32x4 __attribute__((ext_vector_type(4)));
typedef float f32x2 __attribute__((ext_vector_type(2)));
typedef __bf16 bf16x8 __attribute__((ext_vector_type(8)));
typedef __bf16 bf16x2 __attribute__((ext_vector_type(2)));
#define GAS __attribute__((address_space(1)))
#define LAS __attribute__((address_space(3)))

constexpr int D = 640, H = 32, DH = 192, SK = 8192, W = 2048, F = 1536, HD = H * DH;
constexpr int NTHR = 768;
constexpr float LOG2E = 1.4426950408889634f;

constexpr int WIN = 8;
constexpr int KGATE = 24;
constexpr int WOWIN = 3;
constexpr int F_EARLY = 4;
constexpr int TG_QP = 395, TG_Q = 495, TG_WO = 750, TG_F = 760, TG_D = 870, TG_P = 920, TG_O = 990, TG_R = 1130;
constexpr int PF_BYTES = 16128, PF_REP = 3, PF_PER = 400;

constexpr int WS_EPOCH = 0;
constexpr int HS_REP = 2560;
constexpr int WS_HS = 4096;
constexpr int GU_REP = 12288;
constexpr int WS_GU = 32768;
constexpr int WS_YP = WS_GU + 4 * GU_REP;
constexpr int WS_QP = WS_YP + H * D * 4;
constexpr int OP_REC = 1024;
constexpr int WS_OP = WS_QP + H * 8 * DH * 4;
constexpr int WS_END = WS_OP + H * 8 * OP_REC;
constexpr u32 SPIN_CAP = 1u << 21;

constexpr int C_Q = 0, C_KISS = 1, C_VISS = 2, C_O = 3, C_P = 4, C_PUB = 5, C_PF = 8;
constexpr int L_Q = 16;
constexpr int L_P = 112;
constexpr int L_ML = L_P + 384;
constexpr int L_O = L_ML + 8;
constexpr int L_T = L_O + 8 * 192;
constexpr int L_ST = L_T + 2048;
constexpr int L_X = L_ST + 16;
constexpr int L_END = L_X + 8 * 288;

__device__ __forceinline__ u32 fu(float f) { return __float_as_uint(f); }
__device__ __forceinline__ float uf(u32 u) { return __uint_as_float(u); }
__device__ __forceinline__ float lo_f(u32 u) { return uf(u << 16); }
__device__ __forceinline__ float hi_f(u32 u) { return uf(u & 0xffff0000u); }
__device__ __forceinline__ u32 pk_bf16(float lo, float hi) {
  bf16x2 v = {(__bf16)lo, (__bf16)hi};
  return __builtin_bit_cast(u32, v);
}
__device__ __forceinline__ float rbf(float f) { return (float)(__bf16)f; }
__device__ __forceinline__ float dot2(u32 a, u32 b, float acc) {
  return __builtin_amdgcn_fdot2_f32_bf16(__builtin_bit_cast(bf16x2, a), __builtin_bit_cast(bf16x2, b), acc, false);
}
__device__ __forceinline__ f32x4 mfma(u32x4 a, u32x4 b, f32x4 c) {
  return __builtin_amdgcn_mfma_f32_16x16x32_bf16(__builtin_bit_cast(bf16x8, a), __builtin_bit_cast(bf16x8, b), c, 0, 0, 0);
}
__device__ __forceinline__ u32x4 sel(u32 v, u32x4 m) { return u32x4{v & m[0], v & m[1], v & m[2], v & m[3]}; }
template <int CTRL> __device__ __forceinline__ float dpp(float x) {
  return __builtin_bit_cast(float, __builtin_amdgcn_mov_dpp(__builtin_bit_cast(int, x), CTRL, 0xf, 0xf, true));
}
template <int CTRL> __device__ __forceinline__ u32 dppu(u32 x) { return (u32)__builtin_amdgcn_mov_dpp((int)x, CTRL, 0xf, 0xf, true); }
constexpr int XOR1 = 0xB1, XOR2 = 0x4E, HMIR = 0x141, ROR8 = 0x128;
__device__ __forceinline__ float sum8(float x) { x += dpp<XOR1>(x); x += dpp<XOR2>(x); x += dpp<HMIR>(x); return x; }
__device__ __forceinline__ float max8(float x) {
  float r;
  asm volatile("s_nop 1\n\tv_max_f32_dpp %0, %1, %1 quad_perm:[1,0,3,2] row_mask:0xf bank_mask:0xf\n\t"
               "s_nop 1\n\tv_max_f32_dpp %0, %0, %0 quad_perm:[2,3,0,1] row_mask:0xf bank_mask:0xf\n\t"
               "s_nop 1\n\tv_max_f32_dpp %0, %0, %0 row_half_mirror row_mask:0xf bank_mask:0xf" : "=&v"(r) : "v"(x));
  return r;
}
__device__ __forceinline__ float max16(float x) {
  float r;
  asm volatile("s_nop 1\n\tv_max_f32_dpp %0, %1, %1 quad_perm:[1,0,3,2] row_mask:0xf bank_mask:0xf\n\t"
               "s_nop 1\n\tv_max_f32_dpp %0, %0, %0 quad_perm:[2,3,0,1] row_mask:0xf bank_mask:0xf\n\t"
               "s_nop 1\n\tv_max_f32_dpp %0, %0, %0 row_half_mirror row_mask:0xf bank_mask:0xf\n\t"
               "s_nop 1\n\tv_max_f32_dpp %0, %0, %0 row_ror:8 row_mask:0xf bank_mask:0xf\n\ts_nop 1" : "=&v"(r) : "v"(x));
  return r;
}
__device__ __forceinline__ float tr32(float a, float b) {
  auto t = __builtin_amdgcn_permlane32_swap(fu(a), fu(b), false, false);
  return uf(t[0]) + uf(t[1]);
}
__device__ __forceinline__ float tr16(float a, float b) {
  auto t = __builtin_amdgcn_permlane16_swap(fu(a), fu(b), false, false);
  return uf(t[0]) + uf(t[1]);
}
__device__ __forceinline__ float xrow_sum(float x) { x = tr16(x, x); return tr32(x, x); }
__device__ __forceinline__ float xrow_max(float x) {
  auto s = __builtin_amdgcn_permlane16_swap(fu(x), fu(x), false, false);
  x = fmaxf(uf(s[0]), uf(s[1]));
  auto t = __builtin_amdgcn_permlane32_swap(fu(x), fu(x), false, false);
  return fmaxf(uf(t[0]), uf(t[1]));
}
__device__ __forceinline__ float sum64(float x) { x = sum8(x); x += dpp<ROR8>(x); return xrow_sum(x); }
__device__ __forceinline__ f32x4 sum_r8(f32x4 t) {
  float w = tr16(tr32(t[0], t[1]), tr32(t[2], t[3]));
  w += dpp<ROR8>(w);
  auto sa = __builtin_amdgcn_permlane16_swap(fu(w), fu(w), false, false);
  auto se = __builtin_amdgcn_permlane32_swap(sa[0], sa[0], false, false);
  auto so = __builtin_amdgcn_permlane32_swap(sa[1], sa[1], false, false);
  return f32x4{uf(se[0]), uf(se[1]), uf(so[0]), uf(so[1])};
}

typedef __amdgpu_buffer_rsrc_t rsrc_t;
__device__ __forceinline__ rsrc_t mkrs(const void* p, int bytes) { return __builtin_amdgcn_make_buffer_rsrc((void*)p, 0, bytes, 0x00020000); }
__device__ __forceinline__ u32x4 ld_nt(rsrc_t rs, int off) { return __builtin_amdgcn_raw_buffer_load_b128(rs, off, 0, 2); }
__device__ __forceinline__ u32x4 ld_sc1(rsrc_t rs, int off) { return __builtin_amdgcn_raw_buffer_load_b128(rs, off, 0, 16); }
__device__ __forceinline__ u32x2 ld_sc1_64(rsrc_t rs, int off) { return __builtin_amdgcn_raw_buffer_load_b64(rs, off, 0, 16); }
__device__ __forceinline__ void st_sc1(rsrc_t rs, int off, u32x4 v) { __builtin_amdgcn_raw_buffer_store_b128(v, rs, off, 0, 16); }
template <int N> __device__ __forceinline__ void vmw() { asm volatile("s_waitcnt vmcnt(%0)" :: "i"(N) : "memory"); }

__device__ __forceinline__ u32x4 pack4(f32x4 v, u32 tag) {
  const u32 a = fu(v[0]) + 0x80u, b = fu(v[1]) + 0x80u, c = fu(v[2]) + 0x80u, d = fu(v[3]) + 0x80u;
  return u32x4{__builtin_amdgcn_perm(a, b, 0x07060503u), __builtin_amdgcn_perm(b, tag, 0x06050100u),
               __builtin_amdgcn_perm(c, d, 0x07060503u), __builtin_amdgcn_perm(d, tag, 0x06050100u)};
}
__device__ __forceinline__ f32x4 unpack4(u32x4 w) {
  return f32x4{uf(w[0] & 0xffffff00u), uf(__builtin_amdgcn_perm(w[0], w[1], 0x0403020cu)),
               uf(w[2] & 0xffffff00u), uf(__builtin_amdgcn_perm(w[2], w[3], 0x0403020cu))};
}
__device__ __forceinline__ bool ok4(u32x4 w, u32 tag) { return (((w[1] ^ tag) | (w[3] ^ tag)) & 0xffffu) == 0u; }

#define CBAR() asm volatile("" ::: "memory")
#define PIN(x) asm volatile("" : "+v"(x))
#define LGKM0() asm volatile("s_waitcnt lgkmcnt(0)" ::: "memory")
#define WG_BARRIER() asm volatile("s_waitcnt lgkmcnt(0)\n\ts_barrier" ::: "memory")
#define SCHED() __builtin_amdgcn_sched_barrier(0)

__device__ __forceinline__ void lds_wait_ge(volatile LAS u32* w, u32 n) {
#pragma nounroll
  for (u32 sp = 0; sp < SPIN_CAP; ++sp) {
    if (*w >= n) break;
    __builtin_amdgcn_s_sleep(1);
  }
  CBAR();
}
__device__ __forceinline__ void wait_until(u64 t) {
#pragma nounroll
  for (u32 sp = 0; sp < 4096; ++sp) {
    if (__builtin_amdgcn_s_memrealtime() >= t) break;
    __builtin_amdgcn_s_sleep(1);
  }
  CBAR();
}
__device__ __forceinline__ void lds_inc(LAS u32* w, int lane) {
  CBAR();
  if (lane == 0) __hip_atomic_fetch_add(w, 1u, __ATOMIC_RELAXED, __HIP_MEMORY_SCOPE_WORKGROUP);
  CBAR();
}

__global__ void __launch_bounds__(NTHR, 3)
fused_block(const u16* __restrict__ x_, const u16* __restrict__ kc_, const u16* __restrict__ vc_, const u16* __restrict__ wq_,
            const u16* __restrict__ wo_, const u16* __restrict__ wg_, const u16* __restrict__ wu_, const u16* __restrict__ wd_,
            u16* __restrict__ out_, unsigned char* ws_) {
  u64 pc0; asm volatile("s_getpc_b64 %0" : "=s"(pc0));
  asm volatile("" :: "s"(x_), "s"(kc_), "s"(vc_), "s"(wq_), "s"(wo_), "s"(wg_), "s"(wu_), "s"(wd_), "s"(out_), "s"(ws_));
  u32 epw;
  asm volatile("s_load_dword %0, %1, 0x0\n\ts_nop 0" : "=&s"(epw) : "s"(ws_) : "memory");
  __shared__ __attribute__((aligned(16))) u32 smem[L_END];
  LAS u32* L = (LAS u32*)smem;
  volatile LAS u32* LV = (volatile LAS u32*)smem;
  const int tid = threadIdx.x, lane = tid & 63, wave = __builtin_amdgcn_readfirstlane(tid >> 6);
  const int b = blockIdx.x;
  const int xcd = b & 7, idx = b >> 3;
  const int h = xcd * 4 + (idx >> 3), s = idx & 7;
  const rsrc_t rs = mkrs(ws_, WS_END);
  const int r8 = lane >> 3, c8 = lane & 7;
  const u64 tstart = __builtin_amdgcn_s_memrealtime();
  u32x4 msk;
#pragma unroll
  for (int d = 0; d < 4; ++d) msk[d] = (d == (c8 >> 1)) ? ((c8 & 1) ? 0xffff0000u : 0x0000ffffu) : 0u;
  u32 tag;
#define FIRST_BARRIER() do { asm volatile("s_waitcnt lgkmcnt(0)\n\ts_barrier\n\ts_add_u32 %0, %1, 1" : "=s"(tag) : "s"(epw) : "memory", "scc"); } while (0)
  const int g0 = 36 * s + (s > 3 ? s - 3 : 0), ng = s >= 3 ? 37 : 36;
  const size_t kvoff = ((size_t)h * SK + (SK - W) + g0 * 8) * DH;

  if (wave < 8 && s == 7) {
    if (wave == 4 && idx >= 16) {
      const rsrc_t rc = mkrs((const void*)(pc0 & ~127ull), PF_BYTES);
      const int off = (((idx >> 3) - 2) * 64 + lane) * 128;
      u32 t0 = __builtin_amdgcn_raw_buffer_load_b32(rc, off, 0, 2);
      FIRST_BARRIER();
      asm volatile("" :: "v"(t0));
#pragma nounroll
      for (int r = 1; r < PF_REP; ++r) {
        wait_until(tstart + r * PF_PER);
        u32 t1 = __builtin_amdgcn_raw_buffer_load_b32(rc, off, 0, 2);
        asm volatile("" :: "v"(t1));
      }
    } else {
      if (wave == 5) {
        const size_t win = ((size_t)h * SK + (SK - W)) * DH * 2;
        const int q4 = idx >> 3;
        const GAS char* p;
        if (lane < 13) p = (const GAS char*)vc_ + win + (lane < 12 ? lane * 65536 : W * DH * 2 - 64);
        else if (lane < 26) p = (const GAS char*)kc_ + win + (lane < 25 ? (lane - 13) * 65536 : W * DH * 2 - 64);
        else if (lane < 31) p = (const GAS char*)wo_ + (size_t)h * DH * D * 2 + (lane < 30 ? (lane - 26) * 65536 : DH * D * 2 - 64);
        else if (lane < 39) p = (const GAS char*)wg_ + q4 * 491520 + (lane - 31) * 61440;
        else if (lane < 47) p = (const GAS char*)wu_ + q4 * 491520 + (lane - 39) * 61440;
        else if (lane < 55) p = (const GAS char*)wd_ + q4 * 491520 + (lane - 47) * 61440;
        else p = (const GAS char*)out_;
        u32 t0 = __builtin_nontemporal_load((const GAS u32*)p);
        FIRST_BARRIER();
        asm volatile("" :: "v"(t0));
      } else {
        FIRST_BARRIER();
      }
    }
  } else if (wave < 4) {
    __builtin_amdgcn_s_setprio(2);
    const rsrc_t rk = mkrs(kc_ + kvoff, ng * 3072);
    const int loff = wave * 3072 + r8 * 384 + c8 * 16;
    u32x4 kr[30];
#pragma unroll
    for (int i = 0; i < 30; ++i) {
      if (i == WIN) FIRST_BARRIER();
      if (i >= WIN) vmw<WIN - 1>();
      if (i == KGATE) lds_inc(L + C_KISS, lane);
      kr[i] = ld_nt(rk, loff + (i / 3) * 12288 + (i % 3) * 128);
      SCHED();
    }
    float dd[10];
    const bool sbok = c8 == 0 || (c8 == 1 && 36 + wave < ng);
    int padr = L_P + wave * 96 + r8 * 12 + c8, padr2 = c8 < 2 ? padr + 8 : L_T + lane;
    PIN(padr); PIN(padr2);
#pragma unroll
    for (int g = 0; g < 10; ++g) { dd[g] = 0.f; PIN(dd[g]); }
    lds_wait_ge(LV + C_Q, 3);
    u32x4 qp[3];
#pragma unroll
    for (int t = 0; t < 3; ++t) qp[t] = *(const LAS u32x4*)(L + L_Q + t * 32 + c8 * 4);
#pragma unroll
    for (int e = 0; e < 12; ++e) {
#pragma unroll
      for (int g = 0; g < 10; ++g) dd[g] = dot2(kr[g * 3 + e / 4][e % 4], qp[e / 4][e % 4], dd[g]);
      SCHED();
    }
    const bool b0 = c8 & 1, b1 = c8 & 2, b2 = c8 & 4;
    float e4[4], e2[2];
#pragma unroll
    for (int i = 0; i < 4; ++i) { const float keep = b2 ? dd[i + 4] : dd[i], give = b2 ? dd[i] : dd[i + 4]; e4[i] = keep + dpp<HMIR>(give); }
#pragma unroll
    for (int i = 0; i < 2; ++i) { const float keep = b1 ? e4[i + 2] : e4[i], give = b1 ? e4[i] : e4[i + 2]; e2[i] = keep + dpp<XOR2>(give); }
    float sa = (b0 ? e2[1] : e2[0]) + dpp<XOR1>(b0 ? e2[0] : e2[1]);
    float sb = (b0 ? dd[9] : dd[8]) + dpp<XOR1>(b0 ? dd[8] : dd[9]);
    sb += dpp<XOR2>(sb);
    sb += dpp<0x1B>(dpp<HMIR>(sb));
    sa = rbf(rbf(sa) * 0.07216878364870323f);
    sb = rbf(rbf(sb) * 0.07216878364870323f);
    sb = sbok ? sb : -INFINITY;
    const float m = xrow_max(max16(fmaxf(sa, sb)));
    const float pa = __builtin_amdgcn_exp2f((sa - m) * LOG2E), pb = __builtin_amdgcn_exp2f((sb - m) * LOG2E);
    L[padr] = pk_bf16(pa, pa);
    L[padr2] = pk_bf16(pb, pb);
    CBAR();
    if (lane == 0) LV[C_PF + wave] = 1u;
    const float l = sum64(pa + pb);
    if (lane == 0) { L[L_ML + wave] = fu(m); L[L_ML + 4 + wave] = fu(l); }
    lds_inc(L + C_P, lane);
  } else if (wave < 8) {
    __builtin_amdgcn_s_setprio(1);
    const int w = wave - 4;
    const rsrc_t rv = mkrs(vc_ + kvoff, ng * 3072);
    const int loff = w * 3072 + r8 * 384 + c8 * 16;
    FIRST_BARRIER();
    lds_wait_ge(LV + C_KISS, 4);
    u32x4 vr[30];
#pragma unroll
    for (int i = 0; i < 30; ++i) {
      const int j = (i + 27) % 30;
      if (i >= WIN) vmw<WIN - 1>();
      if (i == 29) lds_inc(L + C_VISS, lane);
      vr[j] = ld_nt(rv, loff + (j / 3) * 12288 + (j % 3) * 128);
      SCHED();
    }
    int wadr = ((lane >> 5) == ((lane >> 3) & 1) ? L_O + (w * 2 + (lane >> 5)) * 192 : L_T + 64 + w * 192) + c8 * 8 + ((lane >> 4) & 1) * 4;
    int radr = L_O + 24 * r8 + 4 * (c8 < 6 ? c8 : 0);
    int sadr = WS_OP + (h * 8 + s) * OP_REC + lane * 16;
    u32 tadr = (u32)(uintptr_t)(L + C_O), one = 1u;
    PIN(wadr); PIN(radr); PIN(sadr); PIN(tadr); PIN(one);
    lds_wait_ge(LV + C_PF + w, 1);
    const LAS u32* pr = L + L_P + w * 96 + r8 * 12;
    const u32x4 p0 = *(const LAS u32x4*)(pr), p1 = *(const LAS u32x4*)(pr + 4);
    const u32x2 p2 = *(const LAS u32x2*)(pr + 8);
    float M, Ls, fw;
    f32x4 acc[2][3];
#pragma unroll
    for (int gg = 0; gg < 10; ++gg) {
      const int g = (gg + 9) % 10;
      if (gg == 5) {
        SCHED();
        lds_wait_ge(LV + C_P, 4);
        const f32x4 mm = *(const LAS f32x4*)(L + L_ML), ll = *(const LAS f32x4*)(L + L_ML + 4);
        M = fmaxf(fmaxf(mm[0], mm[1]), fmaxf(mm[2], mm[3]));
        f32x4 ff;
#pragma unroll
        for (int k = 0; k < 4; ++k) ff[k] = __builtin_amdgcn_exp2f((mm[k] - M) * LOG2E);
        Ls = (ff[0] * ll[0] + ff[1] * ll[1]) + (ff[2] * ll[2] + ff[3] * ll[3]);
        fw = w == 0 ? ff[0] : (w == 1 ? ff[1] : (w == 2 ? ff[2] : ff[3]));
        PIN(M); PIN(Ls); PIN(fw);
      }
      const u32x4 A = sel(g < 4 ? p0[g & 3] : (g < 8 ? p1[g & 3] : p2[g & 1]), msk);
#pragma unroll
      for (int t = 0; t < 3; ++t) {
        if (gg == 9) acc[0][t] = mfma(A, vr[g * 3 + t], acc[0][t] + acc[1][t]);
        else acc[gg & 1][t] = mfma(A, vr[g * 3 + t], gg < 2 ? f32x4{0.f, 0.f, 0.f, 0.f} : acc[gg & 1][t]);
      }
    }
#pragma unroll
    for (int t = 0; t < 3; ++t) *(LAS f32x4*)(L + wadr + t * 64) = acc[0][t] * fw;
    u32 old;
    asm volatile("s_mov_b64 exec, 1\n\tds_add_rtn_u32 %0, %1, %2\n\ts_mov_b64 exec, -1\n\ts_waitcnt lgkmcnt(0)" : "=&v"(old) : "v"(tadr), "v"(one) : "memory");
    if (__builtin_amdgcn_readfirstlane(old) == 3) {
      const u64 tnow = __builtin_amdgcn_s_memrealtime();
      f32x4 pt[8];
#pragma unroll
      for (int k = 0; k < 8; ++k) pt[k] = *(const LAS f32x4*)(L + radr + k * 192);
      SCHED();
      f32x4 o = ((pt[0] + pt[1]) + (pt[2] + pt[3])) + ((pt[4] + pt[5]) + (pt[6] + pt[7]));
      if (c8 == 6) { o[0] = M; o[1] = Ls; }
      u32x4 pk = pack4(o, tag);
      PIN(pk);
      if (tnow < tstart + TG_P) wait_until(tstart + TG_P);
      st_sc1(rs, sadr, pk);
      if (lane == 0) LV[C_PUB] = 1u;
    }
    return;
  } else {
    const int a = wave - 8;
    __builtin_amdgcn_s_setprio(3);
    if (a < 3) {
      const GAS char* xb = (const GAS char*)x_;
      const u32x4 xa = *(const GAS u32x4*)(xb + lane * 16);
      const u32 xc = *(const GAS u32*)(xb + 1024 + lane * 4);
      const GAS u32* xr = (const GAS u32*)(xb + 160 * s + 20 * r8);
      u32 xo[5];
#pragma unroll
      for (int i = 0; i < 5; ++i) xo[i] = xr[i];
      const rsrc_t rq = mkrs(wq_ + (size_t)80 * s * HD + h * DH + a * 64, 79 * HD * 2 + 128);
      const int qoff = 10 * r8 * (HD * 2) + c8 * 16;
      u32x4 wr[10];
#pragma unroll
      for (int g = 0; g < 10; ++g) wr[g] = ld_nt(rq, qoff + g * (HD * 2));
      SCHED();
      FIRST_BARRIER();
      SCHED();
      const float xv[10] = {lo_f(xa[0]), hi_f(xa[0]), lo_f(xa[1]), hi_f(xa[1]), lo_f(xa[2]), hi_f(xa[2]), lo_f(xa[3]), hi_f(xa[3]), lo_f(xc), hi_f(xc)};
      float sm = 0.f;
#pragma unroll
      for (int i = 0; i < 10; ++i) sm += xv[i];
      const float mean = sum64(sm) * (1.0f / D);
      float sq = 0.f;
#pragma unroll
      for (int i = 0; i < 10; ++i) { const float dd = xv[i] - mean; sq += dd * dd; }
      const float var = rbf(sum64(sq) * (1.0f / D));
      const float mb = rbf(mean);
      const float rinv = 1.0f / rbf(__builtin_sqrtf(rbf(var + 1e-5f)));
      f32x4 acc = {0.f, 0.f, 0.f, 0.f};
#pragma unroll
      for (int g = 0; g < 10; ++g) {
        const float xg = (g & 1) ? hi_f(xo[g >> 1]) : lo_f(xo[g >> 1]);
        const float xn = rbf(xg - mb) * rinv;
        acc = mfma(sel(pk_bf16(xn, xn), msk), wr[g], acc);
      }
#pragma unroll
      for (int i = 0; i < 4; ++i) {
        const float up = dpp<ROR8>(acc[i]);
        auto t = __builtin_amdgcn_permlane32_swap(fu(up), fu(up), false, false);
        acc[i] += uf(t[1]);
      }
      u32x4 pk = pack4(acc, tag);
      PIN(pk);
      wait_until(tstart + TG_QP);
      if (lane < 32 && (lane & 8) == 0) st_sc1(rs, WS_QP + (h * 8 + s) * (DH * 4) + a * 256 + (c8 + 8 * (lane >> 4)) * 16, pk);
      if (s < 7) {
        const int gb = WS_QP + (h * 8 + (lane >> 4)) * (DH * 4) + a * 256 + (lane & 15) * 16;
        u32x4 v[2];
        bool ok[2] = {false, false};
        wait_until(tstart + TG_Q);
#pragma nounroll
        for (u32 sp = 0; sp < SPIN_CAP; ++sp) {
          CBAR();
#pragma unroll
          for (int i = 0; i < 2; ++i) if (!ok[i]) v[i] = ld_sc1(rs, gb + i * (4 * DH * 4));
#pragma unroll
          for (int i = 0; i < 2; ++i) ok[i] = ok4(v[i], tag);
          if (__all(ok[0] & ok[1])) break;
          __builtin_amdgcn_s_sleep(2);
        }
        f32x4 q = unpack4(v[0]) + unpack4(v[1]);
#pragma unroll
        for (int i = 0; i < 4; ++i) q[i] = xrow_sum(q[i]);
        if (lane < 16) *(LAS u32x2*)(L + L_Q + a * 32 + 4 * c8 + 2 * (lane >> 3)) = u32x2{pk_bf16(q[0], q[1]), pk_bf16(q[2], q[3])};
        lds_inc(L + C_Q, lane);
      }
    } else {
      if (lane < 16) L[lane] = 0u;
      FIRST_BARRIER();
    }
  }
  __builtin_amdgcn_s_setprio(0);

  if (s == 7) {
    const int j4 = idx >> 3;
    const int rI = j4 < 2 ? 2 * xcd + j4 : 14 + 2 * xcd + j4;
    if (rI < 20) {
      const int dt = rI;
      const int r16 = lane >> 2, c4 = lane & 3;
      u32 dw[4][8];
      {
        const rsrc_t rd = mkrs(wd_ + (size_t)128 * wave * D + dt * 32, 127 * D * 2 + 64);
        const int doff = 8 * r16 * (D * 2) + c4 * 16;
        wait_until(tstart + TG_D);
        u32x4 t[8];
#pragma unroll
        for (int i = 0; i < 8; ++i) { t[i] = ld_nt(rd, doff + i * (D * 2)); SCHED(); }
#pragma unroll
        for (int k = 0; k < 4; ++k)
#pragma unroll
          for (int d = 0; d < 4; ++d) {
            dw[k][2 * d] = __builtin_amdgcn_perm(t[2 * k + 1][d], t[2 * k][d], 0x05040100u);
            dw[k][2 * d + 1] = __builtin_amdgcn_perm(t[2 * k + 1][d], t[2 * k][d], 0x07060302u);
          }
#pragma unroll
        for (int k = 0; k < 4; ++k)
#pragma unroll
          for (int c = 0; c < 8; ++c) PIN(dw[k][c]);
      }
      float ac[8];
#pragma unroll
      for (int c = 0; c < 8; ++c) { ac[c] = 0.f; PIN(ac[c]); }
      int wadr = L_T + (wave * 4 + ((lane >> 2) & 3)) * 32 + c4 * 8 + (lane >> 5) * 4 + ((lane >> 4) & 1) * 2;
      int radr = L_T + r8 * 192 + c8 * 4;
      int oadr = dt * 64 + c8 * 8;
      PIN(wadr); PIN(radr); PIN(oadr);
      const rsrc_t ro = mkrs(out_, D * 2);
      u32x2 gv, uv;
      u32x4 hv = {0u, tag, 0u, tag};
      {
        const int o0 = WS_GU + ((rI >> 1) & 3) * GU_REP + (64 * wave + lane) * 8;
        const int oh = WS_HS + xcd * HS_REP + (dt * 16 + c8 * 2) * 8;
#pragma nounroll
        for (u32 sp = 0; sp < SPIN_CAP; ++sp) {
          CBAR();
          gv = ld_sc1_64(rs, o0); uv = ld_sc1_64(rs, o0 + 6144);
          if (wave == 0) hv = ld_sc1(rs, oh);
          if (__all(((gv[1] ^ tag) | (uv[1] ^ tag) | (hv[1] ^ tag) | (hv[3] ^ tag)) == 0u)) break;
        }
      }
      auto glu = [](float g, float u) -> float { return rbf(__builtin_amdgcn_rcpf(1.0f + __builtin_amdgcn_exp2f(-g * LOG2E))) * u; };
      const u32 ha = pk_bf16(glu(lo_f(gv[0]), lo_f(uv[0])), glu(hi_f(gv[0]), hi_f(uv[0])));
      const u32 hh[4] = {dppu<0x00>(ha), dppu<0x55>(ha), dppu<0xAA>(ha), dppu<0xFF>(ha)};
#pragma unroll
      for (int k = 0; k < 4; ++k)
#pragma unroll
        for (int c = 0; c < 8; ++c) ac[c] = dot2(dw[k][c], hh[k], ac[c]);
      float b4[4];
#pragma unroll
      for (int c = 0; c < 4; ++c) b4[c] = tr32(ac[c], ac[c + 4]);
      *(LAS f32x2*)(L + wadr) = f32x2{tr16(b4[0], b4[2]), tr16(b4[1], b4[3])};
      WG_BARRIER();
      if (wave == 0) {
        f32x4 p[6];
#pragma unroll
        for (int k = 0; k < 6; ++k) p[k] = *(const LAS f32x4*)(L + radr + k * 32);
        const f32x4 t = sum_r8(((p[0] + p[1]) + (p[2] + p[3])) + (p[4] + p[5]));
        const u32 d0 = pk_bf16(t[0], t[1]), d1 = pk_bf16(t[2], t[3]);
        const u32x2 ov = {pk_bf16(lo_f(hv[0]) + lo_f(d0), hi_f(hv[0]) + hi_f(d0)), pk_bf16(lo_f(hv[2]) + lo_f(d1), hi_f(hv[2]) + hi_f(d1))};
        if (lane < 8) __builtin_amdgcn_raw_buffer_store_b64(ov, ro, oadr, 0, 16);
        if (rI == 0 && lane == 0) __builtin_amdgcn_raw_buffer_store_b32(tag, rs, WS_EPOCH, 0, 16);
      }
    } else if (rI < 30 && wave < 2) {
      const int ru = (rI - 20) * 2 + wave;
      const u32x2 xp = *(const GAS u32x2*)((const GAS char*)x_ + (ru * 32 + c8 * 4) * 2);
      f32x4 xf = {lo_f(xp[0]), hi_f(xp[0]), lo_f(xp[1]), hi_f(xp[1])};
      int sadr = WS_HS + r8 * HS_REP + (ru * 16 + c8 * 2) * 8;
      PIN(xf); PIN(sadr);
      const int gb = WS_YP + (r8 * D + ru * 32 + c8 * 4) * 4;
      u32x4 v[4];
      bool ok[4] = {false, false, false, false};
      wait_until(tstart + TG_R);
#pragma nounroll
      for (u32 sp = 0; sp < SPIN_CAP; ++sp) {
        CBAR();
#pragma unroll
        for (int i = 0; i < 4; ++i) if (!ok[i]) v[i] = ld_sc1(rs, gb + i * (8 * D * 4));
#pragma unroll
        for (int i = 0; i < 4; ++i) ok[i] = ok4(v[i], tag);
        if (__all(ok[0] & ok[1] & ok[2] & ok[3])) break;
      }
      const f32x4 y = sum_r8((unpack4(v[0]) + unpack4(v[1])) + (unpack4(v[2]) + unpack4(v[3])));
      const u32 a0 = pk_bf16(y[0], y[1]), a1 = pk_bf16(y[2], y[3]);
      st_sc1(rs, sadr, u32x4{pk_bf16(xf[0] + lo_f(a0), xf[1] + hi_f(a0)), tag, pk_bf16(xf[2] + lo_f(a1), xf[3] + hi_f(a1)), tag});
    }
    return;
  }

  const int tw = wave < 4 ? wave : wave - 4;
  if (s < 5) {
    lds_wait_ge(LV + C_VISS, 4);
    const int g4 = lane >> 4, z = lane & 15;
    const rsrc_t ro = mkrs(wo_ + (size_t)(h * DH + 24 * tw) * D + s * 128, 23 * D * 2 + 256);
    const int ooff = 6 * g4 * (D * 2) + z * 16;
    u32x4 wr[6];
    wait_until(tstart + TG_WO);
#pragma unroll
    for (int k = 0; k < 6; ++k) { if (k >= WOWIN) vmw<WOWIN - 1>(); wr[k] = ld_nt(ro, ooff + k * (D * 2)); SCHED(); }
    u32x4 mA, mB;
#pragma unroll
    for (int d = 0; d < 4; ++d) { mA[d] = z < 8 ? msk[d] : 0u; mB[d] = z < 8 ? 0u : msk[d]; }
    int xw = L_X + tw * 288 + r8 * 36 + c8 * 4, xm = L_X + tw * 288 + c8 * 36 + 24, xo = L_X + tw * 288 + c8 * 36 + 3 * r8;
    int wadr = L_T + (tw * 2 + (g4 >> 1)) * 128 + z * 8 + (g4 & 1) * 4;
    int radr = L_T + r8 * 256 + tw * 32 + c8 * 4;
    int sadr = WS_YP + (h * D + s * 128 + tw * 32 + c8 * 4) * 4;
    PIN(mA); PIN(mB); PIN(xw); PIN(xm); PIN(xo); PIN(wadr); PIN(radr); PIN(sadr);
    lds_wait_ge(LV + C_PUB, 1);
    const int gb = WS_OP + (h * 8 + r8) * OP_REC + tw * 128 + c8 * 16;
    u32x4 va = {c8 == 6 ? 0xff800000u : 0u, tag & 0xffffu, 0u, tag & 0xffffu};
    bool ok = r8 == 7;
    wait_until(tstart + TG_O);
#pragma nounroll
    for (u32 sp = 0; sp < SPIN_CAP; ++sp) {
      CBAR();
      if (!ok) va = ld_sc1(rs, gb);
      ok = ok4(va, tag);
      if (__all(ok)) break;
    }
    *(LAS f32x4*)(L + xw) = unpack4(va);
    CBAR();
    const f32x2 ml = *(const LAS f32x2*)(L + xm);
    const float o0 = uf(L[xo]), o1 = uf(L[xo + 1]), o2 = uf(L[xo + 2]);
    SCHED();
    const float f = __builtin_amdgcn_exp2f((ml[0] - max8(ml[0])) * LOG2E);
    float t0 = f * o0, t1 = f * o1, t2 = f * o2, t3 = f * ml[1];
    t0 += dpp<XOR1>(t0); t1 += dpp<XOR1>(t1); t2 += dpp<XOR1>(t2); t3 += dpp<XOR1>(t3);
    t3 += dpp<XOR2>(t3); t0 += dpp<XOR2>(t0); t1 += dpp<XOR2>(t1); t2 += dpp<XOR2>(t2);
    t3 += dpp<HMIR>(t3); t0 += dpp<HMIR>(t0); t1 += dpp<HMIR>(t1); t2 += dpp<HMIR>(t2);
    const float li = __builtin_amdgcn_rcpf(t3);
    t0 *= li; t1 *= li; t2 *= li;
    const u32 v0 = pk_bf16(t0, t0), v1 = pk_bf16(t1, t1), v2 = pk_bf16(t2, t2);
    const f32x4 zero = {0.f, 0.f, 0.f, 0.f};
    f32x4 aA = mfma(sel(v0, mA), wr[0], zero), aB = mfma(sel(v0, mB), wr[3], zero);
    aA = mfma(sel(v1, mA), wr[1], aA); aB = mfma(sel(v1, mB), wr[4], aB);
    aA = mfma(sel(v2, mA), wr[2], aA);
    aA = mfma(sel(v2, mB), wr[5], aA);
    *(LAS f32x4*)(L + wadr) = aA + aB;
    WG_BARRIER();
    if (tw < 4) {
      const f32x4 t = sum_r8(*(const LAS f32x4*)(L + radr) + *(const LAS f32x4*)(L + radr + 128));
      const u32x4 pk = pack4(t, tag);
      if (lane < 8) st_sc1(rs, sadr, pk);
    }
    return;
  }

  const int fi = (idx >> 3) * 2 + (s - 5);
  if (fi < 6) {
    const int ft = xcd * 6 + fi, mat = ft & 1, tile = ft >> 1;
    lds_wait_ge(LV + C_VISS, 4);
    u32 gw[5][8];
    {
      const rsrc_t rg = mkrs((mat ? wu_ : wg_) + (size_t)80 * tw * F + tile * 64, 79 * F * 2 + 128);
      const int goff = 10 * r8 * (F * 2) + c8 * 16;
      u32x4 t[10];
      wait_until(tstart + TG_F);
#pragma unroll
      for (int i = 0; i < 10; ++i) {
        if (i == F_EARLY) { SCHED(); lds_wait_ge(LV + C_PUB, 1); SCHED(); }
        t[i] = ld_nt(rg, goff + i * (F * 2));
        SCHED();
      }
      SCHED();
#pragma unroll
      for (int i = 0; i < 5; ++i)
#pragma unroll
        for (int d = 0; d < 4; ++d) {
          gw[i][2 * d] = __builtin_amdgcn_perm(t[2 * i + 1][d], t[2 * i][d], 0x05040100u);
          gw[i][2 * d + 1] = __builtin_amdgcn_perm(t[2 * i + 1][d], t[2 * i][d], 0x07060302u);
        }
#pragma unroll
      for (int i = 0; i < 5; ++i)
#pragma unroll
        for (int c = 0; c < 8; ++c) PIN(gw[i][c]);
    }
    int wadr = L_T + (tw * 2 + ((lane >> 3) & 1)) * 64 + c8 * 8 + (lane >> 5) * 4 + ((lane >> 4) & 1) * 2;
    int radr = L_T + (lane >> 4) * 256 + (lane & 15) * 4;
    auto gemv = [&](const u32 (&hn)[5], float (&ac)[8]) {
#pragma unroll
      for (int i = 0; i < 5; ++i)
#pragma unroll
        for (int c = 0; c < 8; ++c) ac[c] = dot2(gw[i][c], hn[i], ac[c]);
      float b4[4];
#pragma unroll
      for (int c = 0; c < 4; ++c) b4[c] = tr32(ac[c], ac[c + 4]);
      *(LAS f32x2*)(L + wadr) = f32x2{tr16(b4[0], b4[2]), tr16(b4[1], b4[3])};
    };
    auto total = [&]() -> float {
      const f32x4 g = (*(const LAS f32x4*)(L + radr) + *(const LAS f32x4*)(L + radr + 64)) + (*(const LAS f32x4*)(L + radr + 128) + *(const LAS f32x4*)(L + radr + 192));
      return tr16(tr32(g[0], g[2]), tr32(g[1], g[3]));
    };
    float ac[8];
    {
      const u32 ones[5] = {0x3f803f80u, 0x3f803f80u, 0x3f803f80u, 0x3f803f80u, 0x3f803f80u};
#pragma unroll
      for (int c = 0; c < 8; ++c) ac[c] = 0.f;
      gemv(ones, ac);
    }
    WG_BARRIER();
    float cw = 0.f;
    if (tw == 0) cw = total();
    PIN(cw);
    WG_BARRIER();
#pragma unroll
    for (int c = 0; c < 8; ++c) { ac[c] = 0.f; PIN(ac[c]); }
    float e5 = c8 < 5 ? 1.0f : 0.0f;
    int sadr = WS_GU + (lane >> 4) * GU_REP + mat * 6144 + tile * 256 + (lane & 15) * 16;
    int stw = L_ST + tw, str = L_ST + c8;
    PIN(e5); PIN(sadr); PIN(wadr); PIN(radr); PIN(stw); PIN(str);
    u32x2 hv;
    {
      const int o0 = WS_HS + xcd * HS_REP + (40 * tw + 5 * r8 + (c8 < 5 ? c8 : 0)) * 8;
#pragma nounroll
      for (u32 sp = 0; sp < SPIN_CAP; ++sp) {
        CBAR();
        hv = ld_sc1_64(rs, o0);
        if (__all(hv[1] == tag)) break;
      }
    }
    u32 hn[5];
    hn[0] = __builtin_amdgcn_ds_swizzle(hv[0], 0x18 | (0 << 5));
    hn[1] = __builtin_amdgcn_ds_swizzle(hv[0], 0x18 | (1 << 5));
    hn[2] = __builtin_amdgcn_ds_swizzle(hv[0], 0x18 | (2 << 5));
    hn[3] = __builtin_amdgcn_ds_swizzle(hv[0], 0x18 | (3 << 5));
    hn[4] = __builtin_amdgcn_ds_swizzle(hv[0], 0x18 | (4 << 5));
    const float pv = lo_f(__builtin_amdgcn_readfirstlane(hv[0]));
    const float d0 = (lo_f(hv[0]) - pv) * e5, d1 = (hi_f(hv[0]) - pv) * e5;
    float s1 = sum8(d0 + d1), s2 = sum8(d0 * d0 + d1 * d1);
    s1 += dpp<ROR8>(s1); s2 += dpp<ROR8>(s2);
    float ss = tr32(s1, s2);
    ss = tr16(ss, ss);
    gemv(hn, ac);
    {
      auto sw = __builtin_amdgcn_permlane32_swap(fu(ss), fu(ss), false, false);
      const float t1 = uf(sw[0]), t2 = uf(sw[1]);
      if (lane == 0) { L[stw] = fu(pv + t1 * (1.0f / 80.0f)); L[stw + 8] = fu(t2 - t1 * t1 * (1.0f / 80.0f)); }
    }
    WG_BARRIER();
    if (tw == 0) {
      const float mw = uf(L[str]), qw = uf(L[str + 8]);
      const float G = total();
      const float mean = sum8(mw) * 0.125f;
      const float var = rbf(sum8(qw + 80.0f * (mw - mean) * (mw - mean)) * (1.0f / D));
      const float rinv = __builtin_amdgcn_rcpf(rbf(__builtin_amdgcn_sqrtf(rbf(var + 1e-5f))));
      const float g = (G - rbf(mean) * cw) * rinv;
      auto sa = __builtin_amdgcn_permlane16_swap(fu(g), fu(g), false, false);
      const u32 pr = pk_bf16(uf(sa[0]), uf(sa[1]));
      auto sb = __builtin_amdgcn_permlane32_swap(pr, pr, false, false);
      st_sc1(rs, sadr, u32x4{sb[0], tag, sb[1], tag});
    }
  }
}

extern "C" void kernel_launch(void* const* d_in, const int* in_sizes, int n_in,
                              void* d_out, int out_size, void* d_ws, size_t ws_size,
                              hipStream_t stream) {
  hipLaunchKernelGGL(fused_block, dim3(256), dim3(NTHR), 0, stream,
                     (const u16*)d_in[0], (const u16*)d_in[1], (const u16*)d_in[2], (const u16*)d_in[3], (const u16*)d_in[4],
                     (const u16*)d_in[6], (const u16*)d_in[7], (const u16*)d_in[8], (u16*)d_out, (unsigned char*)d_ws);
}
```

```cpp
#include <hip/hip_runtime.h>
#include <hip/hip_bf16.h>
#include <stdint.h>

typedef unsigned short u16;
typedef unsigned u32;
typedef unsigned long long u64;
typedef u32 u32x4 __attribute__((ext_vector_type(4)));
typedef u32 u32x2 __attribute__((ext_vector_type(2)));
typedef float f32x4 __attribute__((ext_vector_type(4)));
typedef float f32x2 __attribute__((ext_vector_type(2)));
typedef __bf16 bf16x8 __attribute__((ext_vector_type(8)));
typedef __bf16 bf16x2 __attribute__((ext_vector_type(2)));
#define GAS __attribute__((address_space(1)))
#define LAS __attribute__((address_space(3)))

constexpr int D = 640, H = 32, DH = 192, SK = 8192, W = 2048, F = 1536, HD = H * DH;
constexpr int NTHR = 768;
constexpr float LOG2E = 1.4426950408889634f;

constexpr int WIN = 8;
constexpr int KGATE = 24;
constexpr int WOWIN = 3;
constexpr int F_EARLY = 4;
constexpr int TG_QP = 395, TG_Q = 495, TG_WO = 775, TG_F = 785, TG_D = 895, TG_P = 945, TG_O = 1015, TG_R = 1155;
constexpr int LI1 = 22, LT1 = 670, LWIN = 14, TL_WO = 820;
constexpr int PF_BYTES = 16640, PF_REP = 3, PF_PER = 400;

constexpr int WS_EPOCH = 0;
constexpr int HS_REP = 2560;
constexpr int WS_HS = 4096;
constexpr int GU_REP = 12288;
constexpr int WS_GU = 32768;
constexpr int WS_YP = WS_GU + 4 * GU_REP;
constexpr int WS_QP = WS_YP + H * D * 4;
constexpr int OP_REC = 1024;
constexpr int WS_OP = WS_QP + H * 8 * DH * 4;
constexpr int WS_END = WS_OP + H * 8 * OP_REC;
constexpr u32 SPIN_CAP = 1u << 21;

constexpr int C_Q = 0, C_KISS = 1, C_VISS = 2, C_O = 3, C_P = 4, C_PUB = 5, C_PF = 8;
constexpr int L_Q = 16;
constexpr int L_P = 112;
constexpr int L_ML = L_P + 384;
constexpr int L_O = L_ML + 8;
constexpr int L_T = L_O + 8 * 192;
constexpr int L_ST = L_T + 2048;
constexpr int L_X = L_ST + 16;
constexpr int L_END = L_X + 8 * 288;

__device__ __forceinline__ u32 fu(float f) { return __float_as_uint(f); }
__device__ __forceinline__ float uf(u32 u) { return __uint_as_float(u); }
__device__ __forceinline__ float lo_f(u32 u) { return uf(u << 16); }
__device__ __forceinline__ float hi_f(u32 u) { return uf(u & 0xffff0000u); }
__device__ __forceinline__ u32 pk_bf16(float lo, float hi) {
  bf16x2 v = {(__bf16)lo, (__bf16)hi};
  return __builtin_bit_cast(u32, v);
}
__device__ __forceinline__ float rbf(float f) { return (float)(__bf16)f; }
__device__ __forceinline__ float dot2(u32 a, u32 b, float acc) {
  return __builtin_amdgcn_fdot2_f32_bf16(__builtin_bit_cast(bf16x2, a), __builtin_bit_cast(bf16x2, b), acc, false);
}
__device__ __forceinline__ f32x4 mfma(u32x4 a, u32x4 b, f32x4 c) {
  return __builtin_amdgcn_mfma_f32_16x16x32_bf16(__builtin_bit_cast(bf16x8, a), __builtin_bit_cast(bf16x8, b), c, 0, 0, 0);
}
__device__ __forceinline__ u32x4 sel(u32 v, u32x4 m) { return u32x4{v & m[0], v & m[1], v & m[2], v & m[3]}; }
template <int CTRL> __device__ __forceinline__ float dpp(float x) {
  return __builtin_bit_cast(float, __builtin_amdgcn_mov_dpp(__builtin_bit_cast(int, x), CTRL, 0xf, 0xf, true));
}
template <int CTRL> __device__ __forceinline__ u32 dppu(u32 x) { return (u32)__builtin_amdgcn_mov_dpp((int)x, CTRL, 0xf, 0xf, true); }
constexpr int XOR1 = 0xB1, XOR2 = 0x4E, HMIR = 0x141, ROR8 = 0x128;
__device__ __forceinline__ float sum8(float x) { x += dpp<XOR1>(x); x += dpp<XOR2>(x); x += dpp<HMIR>(x); return x; }
__device__ __forceinline__ float max8(float x) {
  float r;
  asm volatile("s_nop 1\n\tv_max_f32_dpp %0, %1, %1 quad_perm:[1,0,3,2] row_mask:0xf bank_mask:0xf\n\t"
               "s_nop 1\n\tv_max_f32_dpp %0, %0, %0 quad_perm:[2,3,0,1] row_mask:0xf bank_mask:0xf\n\t"
               "s_nop 1\n\tv_max_f32_dpp %0, %0, %0 row_half_mirror row_mask:0xf bank_mask:0xf" : "=&v"(r) : "v"(x));
  return r;
}
__device__ __forceinline__ float max16(float x) {
  float r;
  asm volatile("s_nop 1\n\tv_max_f32_dpp %0, %1, %1 quad_perm:[1,0,3,2] row_mask:0xf bank_mask:0xf\n\t"
               "s_nop 1\n\tv_max_f32_dpp %0, %0, %0 quad_perm:[2,3,0,1] row_mask:0xf bank_mask:0xf\n\t"
               "s_nop 1\n\tv_max_f32_dpp %0, %0, %0 row_half_mirror row_mask:0xf bank_mask:0xf\n\t"
               "s_nop 1\n\tv_max_f32_dpp %0, %0, %0 row_ror:8 row_mask:0xf bank_mask:0xf\n\ts_nop 1" : "=&v"(r) : "v"(x));
  return r;
}
__device__ __forceinline__ float tr32(float a, float b) {
  auto t = __builtin_amdgcn_permlane32_swap(fu(a), fu(b), false, false);
  return uf(t[0]) + uf(t[1]);
}
__device__ __forceinline__ float tr16(float a, float b) {
  auto t = __builtin_amdgcn_permlane16_swap(fu(a), fu(b), false, false);
  return uf(t[0]) + uf(t[1]);
}
__device__ __forceinline__ float xrow_sum(float x) { x = tr16(x, x); return tr32(x, x); }
__device__ __forceinline__ float xrow_max(float x) {
  auto s = __builtin_amdgcn_permlane16_swap(fu(x), fu(x), false, false);
  x = fmaxf(uf(s[0]), uf(s[1]));
  auto t = __builtin_amdgcn_permlane32_swap(fu(x), fu(x), false, false);
  return fmaxf(uf(t[0]), uf(t[1]));
}
__device__ __forceinline__ float sum64(float x) { x = sum8(x); x += dpp<ROR8>(x); return xrow_sum(x); }
__device__ __forceinline__ f32x4 sum_r8(f32x4 t) {
  float w = tr16(tr32(t[0], t[1]), tr32(t[2], t[3]));
  w += dpp<ROR8>(w);
  auto sa = __builtin_amdgcn_permlane16_swap(fu(w), fu(w), false, false);
  auto se = __builtin_amdgcn_permlane32_swap(sa[0], sa[0], false, false);
  auto so = __builtin_amdgcn_permlane32_swap(sa[1], sa[1], false, false);
  return f32x4{uf(se[0]), uf(se[1]), uf(so[0]), uf(so[1])};
}

typedef __amdgpu_buffer_rsrc_t rsrc_t;
__device__ __forceinline__ rsrc_t mkrs(const void* p, int bytes) { return __builtin_amdgcn_make_buffer_rsrc((void*)p, 0, bytes, 0x00020000); }
__device__ __forceinline__ u32x4 ld_nt(rsrc_t rs, int off) { return __builtin_amdgcn_raw_buffer_load_b128(rs, off, 0, 2); }
__device__ __forceinline__ u32x4 ld_sc1(rsrc_t rs, int off) { return __builtin_amdgcn_raw_buffer_load_b128(rs, off, 0, 16); }
__device__ __forceinline__ u32x2 ld_sc1_64(rsrc_t rs, int off) { return __builtin_amdgcn_raw_buffer_load_b64(rs, off, 0, 16); }
__device__ __forceinline__ void st_sc1(rsrc_t rs, int off, u32x4 v) { __builtin_amdgcn_raw_buffer_store_b128(v, rs, off, 0, 16); }
template <int N> __device__ __forceinline__ void vmw() { asm volatile("s_waitcnt vmcnt(%0)" :: "i"(N) : "memory"); }

__device__ __forceinline__ u32x4 pack4(f32x4 v, u32 tag) {
  const u32 a = fu(v[0]) + 0x80u, b = fu(v[1]) + 0x80u, c = fu(v[2]) + 0x80u, d = fu(v[3]) + 0x80u;
  return u32x4{__builtin_amdgcn_perm(a, b, 0x07060503u), __builtin_amdgcn_perm(b, tag, 0x06050100u),
               __builtin_amdgcn_perm(c, d, 0x07060503u), __builtin_amdgcn_perm(d, tag, 0x06050100u)};
}
__device__ __forceinline__ f32x4 unpack4(u32x4 w) {
  return f32x4{uf(w[0] & 0xffffff00u), uf(__builtin_amdgcn_perm(w[0], w[1], 0x0403020cu)),
               uf(w[2] & 0xffffff00u), uf(__builtin_amdgcn_perm(w[2], w[3], 0x0403020cu))};
}
__device__ __forceinline__ bool ok4(u32x4 w, u32 tag) { return (((w[1] ^ tag) | (w[3] ^ tag)) & 0xffffu) == 0u; }

#define CBAR() asm volatile("" ::: "memory")
#define PIN(x) asm volatile("" : "+v"(x))
#define LGKM0() asm volatile("s_waitcnt lgkmcnt(0)" ::: "memory")
#define WG_BARRIER() asm volatile("s_waitcnt lgkmcnt(0)\n\ts_barrier" ::: "memory")
#define SCHED() __builtin_amdgcn_sched_barrier(0)

__device__ __forceinline__ void lds_wait_ge(volatile LAS u32* w, u32 n) {
#pragma nounroll
  for (u32 sp = 0; sp < SPIN_CAP; ++sp) {
    if (*w >= n) break;
    __builtin_amdgcn_s_sleep(1);
  }
  CBAR();
}
__device__ __forceinline__ void wait_until(u64 t) {
#pragma nounroll
  for (u32 sp = 0; sp < 4096; ++sp) {
    if (__builtin_amdgcn_s_memrealtime() >= t) break;
    __builtin_amdgcn_s_sleep(1);
  }
  CBAR();
}
__device__ __forceinline__ void lds_inc(LAS u32* w, int lane) {
  CBAR();
  if (lane == 0) __hip_atomic_fetch_add(w, 1u, __ATOMIC_RELAXED, __HIP_MEMORY_SCOPE_WORKGROUP);
  CBAR();
}

__global__ void __launch_bounds__(NTHR, 3)
fused_block(const u16* __restrict__ x_, const u16* __restrict__ kc_, const u16* __restrict__ vc_, const u16* __restrict__ wq_,
            const u16* __restrict__ wo_, const u16* __restrict__ wg_, const u16* __restrict__ wu_, const u16* __restrict__ wd_,
            u16* __restrict__ out_, unsigned char* ws_) {
  u64 pc0; asm volatile("s_getpc_b64 %0" : "=s"(pc0));
  asm volatile("" :: "s"(x_), "s"(kc_), "s"(vc_), "s"(wq_), "s"(wo_), "s"(wg_), "s"(wu_), "s"(wd_), "s"(out_), "s"(ws_));
  u32 epw;
  asm volatile("s_load_dword %0, %1, 0x0\n\ts_nop 0" : "=&s"(epw) : "s"(ws_) : "memory");
  __shared__ __attribute__((aligned(16))) u32 smem[L_END];
  LAS u32* L = (LAS u32*)smem;
  volatile LAS u32* LV = (volatile LAS u32*)smem;
  const int tid = threadIdx.x, lane = tid & 63, wave = __builtin_amdgcn_readfirstlane(tid >> 6);
  const int b = blockIdx.x;
  const int xcd = b & 7, idx = b >> 3;
  const int h = xcd * 4 + (idx >> 3), s = idx & 7;
  const rsrc_t rs = mkrs(ws_, WS_END);
  const int r8 = lane >> 3, c8 = lane & 7;
  const u64 tstart = __builtin_amdgcn_s_memrealtime();
  u32x4 msk;
#pragma unroll
  for (int d = 0; d < 4; ++d) msk[d] = (d == (c8 >> 1)) ? ((c8 & 1) ? 0xffff0000u : 0x0000ffffu) : 0u;
  u32 tag;
#define FIRST_BARRIER() do { asm volatile("s_waitcnt lgkmcnt(0)\n\ts_barrier\n\ts_add_u32 %0, %1, 1" : "=s"(tag) : "s"(epw) : "memory", "scc"); } while (0)
  const int g0 = 36 * s + (s > 3 ? s - 3 : 0), ng = s >= 3 ? 37 : 36;
  const size_t kvoff = ((size_t)h * SK + (SK - W) + g0 * 8) * DH;

  if (wave < 8 && s == 7) {
    if (wave == 4 && idx >= 8) {
      const rsrc_t rc = mkrs((const void*)(pc0 & ~127ull), PF_BYTES);
      const int off = ((3 - (idx >> 3)) * 64 + lane) * 128;
      u32 t0 = __builtin_amdgcn_raw_buffer_load_b32(rc, off, 0, 2);
      FIRST_BARRIER();
      asm volatile("" :: "v"(t0));
#pragma nounroll
      for (int r = 1; r < PF_REP; ++r) {
        wait_until(tstart + r * PF_PER);
        u32 t1 = __builtin_amdgcn_raw_buffer_load_b32(rc, off, 0, 2);
        asm volatile("" :: "v"(t1));
      }
    } else {
      if (wave == 5) {
        const size_t win = ((size_t)h * SK + (SK - W)) * DH * 2;
        const int q4 = idx >> 3;
        const GAS char* p;
        if (lane < 13) p = (const GAS char*)vc_ + win + (lane < 12 ? lane * 65536 : W * DH * 2 - 64);
        else if (lane < 26) p = (const GAS char*)kc_ + win + (lane < 25 ? (lane - 13) * 65536 : W * DH * 2 - 64);
        else if (lane < 31) p = (const GAS char*)wo_ + (size_t)h * DH * D * 2 + (lane < 30 ? (lane - 26) * 65536 : DH * D * 2 - 64);
        else if (lane < 39) p = (const GAS char*)wg_ + q4 * 491520 + (lane - 31) * 61440;
        else if (lane < 47) p = (const GAS char*)wu_ + q4 * 491520 + (lane - 39) * 61440;
        else if (lane < 55) p = (const GAS char*)wd_ + q4 * 491520 + (lane - 47) * 61440;
        else p = (const GAS char*)out_;
        u32 t0 = __builtin_nontemporal_load((const GAS u32*)p);
        FIRST_BARRIER();
        asm volatile("" :: "v"(t0));
      } else {
        FIRST_BARRIER();
      }
    }
  } else if (wave < 4) {
    __builtin_amdgcn_s_setprio(2);
    const rsrc_t rk = mkrs(kc_ + kvoff, ng * 3072);
    const int loff = wave * 3072 + r8 * 384 + c8 * 16;
    u32x4 kr[30];
#pragma unroll
    for (int i = 0; i < 30; ++i) {
      if (i == WIN) FIRST_BARRIER();
      if (i >= WIN) vmw<WIN - 1>();
      if (i == KGATE) lds_inc(L + C_KISS, lane);
      kr[i] = ld_nt(rk, loff + (i / 3) * 12288 + (i % 3) * 128);
      SCHED();
    }
    float dd[10];
    const bool sbok = c8 == 0 || (c8 == 1 && 36 + wave < ng);
    int padr = L_P + wave * 96 + r8 * 12 + c8, padr2 = c8 < 2 ? padr + 8 : L_T + lane;
    PIN(padr); PIN(padr2);
#pragma unroll
    for (int g = 0; g < 10; ++g) { dd[g] = 0.f; PIN(dd[g]); }
    lds_wait_ge(LV + C_Q, 3);
    u32x4 qp[3];
#pragma unroll
    for (int t = 0; t < 3; ++t) qp[t] = *(const LAS u32x4*)(L + L_Q + t * 32 + c8 * 4);
#pragma unroll
    for (int e = 0; e < 12; ++e) {
#pragma unroll
      for (int g = 0; g < 10; ++g) dd[g] = dot2(kr[g * 3 + e / 4][e % 4], qp[e / 4][e % 4], dd[g]);
      SCHED();
    }
    const bool b0 = c8 & 1, b1 = c8 & 2, b2 = c8 & 4;
    float e4[4], e2[2];
#pragma unroll
    for (int i = 0; i < 4; ++i) { const float keep = b2 ? dd[i + 4] : dd[i], give = b2 ? dd[i] : dd[i + 4]; e4[i] = keep + dpp<HMIR>(give); }
#pragma unroll
    for (int i = 0; i < 2; ++i) { const float keep = b1 ? e4[i + 2] : e4[i], give = b1 ? e4[i] : e4[i + 2]; e2[i] = keep + dpp<XOR2>(give); }
    float sa = (b0 ? e2[1] : e2[0]) + dpp<XOR1>(b0 ? e2[0] : e2[1]);
    float sb = (b0 ? dd[9] : dd[8]) + dpp<XOR1>(b0 ? dd[8] : dd[9]);
    sb += dpp<XOR2>(sb);
    sb += dpp<0x1B>(dpp<HMIR>(sb));
    sa = rbf(rbf(sa) * 0.07216878364870323f);
    sb = rbf(rbf(sb) * 0.07216878364870323f);
    sb = sbok ? sb : -INFINITY;
    const float m = xrow_max(max16(fmaxf(sa, sb)));
    const float pa = __builtin_amdgcn_exp2f((sa - m) * LOG2E), pb = __builtin_amdgcn_exp2f((sb - m) * LOG2E);
    L[padr] = pk_bf16(pa, pa);
    L[padr2] = pk_bf16(pb, pb);
    CBAR();
    if (lane == 0) LV[C_PF + wave] = 1u;
    const float l = sum64(pa + pb);
    if (lane == 0) { L[L_ML + wave] = fu(m); L[L_ML + 4 + wave] = fu(l); }
    lds_inc(L + C_P, lane);
  } else if (wave < 8) {
    __builtin_amdgcn_s_setprio(1);
    const int w = wave - 4;
    const rsrc_t rv = mkrs(vc_ + kvoff, ng * 3072);
    const int loff = w * 3072 + r8 * 384 + c8 * 16;
    FIRST_BARRIER();
    lds_wait_ge(LV + C_KISS, 4);
    u32x4 vr[30];
    bool late = false;
#pragma unroll
    for (int i = 0; i < 30; ++i) {
      const int j = (i + 27) % 30;
      if (i == LI1) late = __builtin_amdgcn_s_memrealtime() > tstart + LT1;
      if (i >= WIN) { if (i < LI1 || !late) vmw<WIN - 1>(); else vmw<LWIN - 1>(); }
      if (i == 29) lds_inc(L + C_VISS, lane);
      vr[j] = ld_nt(rv, loff + (j / 3) * 12288 + (j % 3) * 128);
      SCHED();
    }
    int wadr = ((lane >> 5) == ((lane >> 3) & 1) ? L_O + (w * 2 + (lane >> 5)) * 192 : L_T + 64 + w * 192) + c8 * 8 + ((lane >> 4) & 1) * 4;
    int radr = L_O + 24 * r8 + 4 * (c8 < 6 ? c8 : 0);
    int sadr = WS_OP + (h * 8 + s) * OP_REC + lane * 16;
    u32 tadr = (u32)(uintptr_t)(L + C_O), one = 1u;
    PIN(wadr); PIN(radr); PIN(sadr); PIN(tadr); PIN(one);
    lds_wait_ge(LV + C_PF + w, 1);
    const LAS u32* pr = L + L_P + w * 96 + r8 * 12;
    const u32x4 p0 = *(const LAS u32x4*)(pr), p1 = *(const LAS u32x4*)(pr + 4);
    const u32x2 p2 = *(const LAS u32x2*)(pr + 8);
    float M, Ls, fw;
    f32x4 acc[2][3];
#pragma unroll
    for (int gg = 0; gg < 10; ++gg) {
      const int g = (gg + 9) % 10;
      if (gg == 5) {
        SCHED();
        lds_wait_ge(LV + C_P, 4);
        const f32x4 mm = *(const LAS f32x4*)(L + L_ML), ll = *(const LAS f32x4*)(L + L_ML + 4);
        M = fmaxf(fmaxf(mm[0], mm[1]), fmaxf(mm[2], mm[3]));
        f32x4 ff;
#pragma unroll
        for (int k = 0; k < 4; ++k) ff[k] = __builtin_amdgcn_exp2f((mm[k] - M) * LOG2E);
        Ls = (ff[0] * ll[0] + ff[1] * ll[1]) + (ff[2] * ll[2] + ff[3] * ll[3]);
        fw = w == 0 ? ff[0] : (w == 1 ? ff[1] : (w == 2 ? ff[2] : ff[3]));
        PIN(M); PIN(Ls); PIN(fw);
      }
      const u32x4 A = sel(g < 4 ? p0[g & 3] : (g < 8 ? p1[g & 3] : p2[g & 1]), msk);
#pragma unroll
      for (int t = 0; t < 3; ++t) {
        if (gg == 9) acc[0][t] = mfma(A, vr[g * 3 + t], acc[0][t] + acc[1][t]);
        else acc[gg & 1][t] = mfma(A, vr[g * 3 + t], gg < 2 ? f32x4{0.f, 0.f, 0.f, 0.f} : acc[gg & 1][t]);
      }
    }
#pragma unroll
    for (int t = 0; t < 3; ++t) *(LAS f32x4*)(L + wadr + t * 64) = acc[0][t] * fw;
    u32 old;
    asm volatile("s_mov_b64 exec, 1\n\tds_add_rtn_u32 %0, %1, %2\n\ts_mov_b64 exec, -1\n\ts_waitcnt lgkmcnt(0)" : "=&v"(old) : "v"(tadr), "v"(one) : "memory");
    if (__builtin_amdgcn_readfirstlane(old) == 3) {
      const u64 tnow = __builtin_amdgcn_s_memrealtime();
      f32x4 pt[8];
#pragma unroll
      for (int k = 0; k < 8; ++k) pt[k] = *(const LAS f32x4*)(L + radr + k * 192);
      SCHED();
      f32x4 o = ((pt[0] + pt[1]) + (pt[2] + pt[3])) + ((pt[4] + pt[5]) + (pt[6] + pt[7]));
      if (c8 == 6) { o[0] = M; o[1] = Ls; }
      u32x4 pk = pack4(o, tag);
      PIN(pk);
      if (tnow < tstart + TG_P) wait_until(tstart + TG_P);
      st_sc1(rs, sadr, pk);
      if (lane == 0) LV[C_PUB] = 1u;
    }
    return;
  } else {
    const int a = wave - 8;
    __builtin_amdgcn_s_setprio(3);
    if (a < 3) {
      const GAS char* xb = (const GAS char*)x_;
      const u32x4 xa = *(const GAS u32x4*)(xb + lane * 16);
      const u32 xc = *(const GAS u32*)(xb + 1024 + lane * 4);
      const GAS u32* xr = (const GAS u32*)(xb + 160 * s + 20 * r8);
      u32 xo[5];
#pragma unroll
      for (int i = 0; i < 5; ++i) xo[i] = xr[i];
      const rsrc_t rq = mkrs(wq_ + (size_t)80 * s * HD + h * DH + a * 64, 79 * HD * 2 + 128);
      const int qoff = 10 * r8 * (HD * 2) + c8 * 16;
      u32x4 wr[10];
#pragma unroll
      for (int g = 0; g < 10; ++g) wr[g] = ld_nt(rq, qoff + g * (HD * 2));
      SCHED();
      FIRST_BARRIER();
      SCHED();
      const float xv[10] = {lo_f(xa[0]), hi_f(xa[0]), lo_f(xa[1]), hi_f(xa[1]), lo_f(xa[2]), hi_f(xa[2]), lo_f(xa[3]), hi_f(xa[3]), lo_f(xc), hi_f(xc)};
      float sm = 0.f;
#pragma unroll
      for (int i = 0; i < 10; ++i) sm += xv[i];
      const float mean = sum64(sm) * (1.0f / D);
      float sq = 0.f;
#pragma unroll
      for (int i = 0; i < 10; ++i) { const float dd = xv[i] - mean; sq += dd * dd; }
      const float var = rbf(sum64(sq) * (1.0f / D));
      const float mb = rbf(mean);
      const float rinv = 1.0f / rbf(__builtin_sqrtf(rbf(var + 1e-5f)));
      f32x4 acc = {0.f, 0.f, 0.f, 0.f};
#pragma unroll
      for (int g = 0; g < 10; ++g) {
        const float xg = (g & 1) ? hi_f(xo[g >> 1]) : lo_f(xo[g >> 1]);
        const float xn = rbf(xg - mb) * rinv;
        acc = mfma(sel(pk_bf16(xn, xn), msk), wr[g], acc);
      }
#pragma unroll
      for (int i = 0; i < 4; ++i) {
        const float up = dpp<ROR8>(acc[i]);
        auto t = __builtin_amdgcn_permlane32_swap(fu(up), fu(up), false, false);
        acc[i] += uf(t[1]);
      }
      u32x4 pk = pack4(acc, tag);
      PIN(pk);
      wait_until(tstart + TG_QP);
      if (lane < 32 && (lane & 8) == 0) st_sc1(rs, WS_QP + (h * 8 + s) * (DH * 4) + a * 256 + (c8 + 8 * (lane >> 4)) * 16, pk);
      if (s < 7) {
        const int gb = WS_QP + (h * 8 + (lane >> 4)) * (DH * 4) + a * 256 + (lane & 15) * 16;
        u32x4 v[2];
        bool ok[2] = {false, false};
        wait_until(tstart + TG_Q);
#pragma nounroll
        for (u32 sp = 0; sp < SPIN_CAP; ++sp) {
          CBAR();
#pragma unroll
          for (int i = 0; i < 2; ++i) if (!ok[i]) v[i] = ld_sc1(rs, gb + i * (4 * DH * 4));
#pragma unroll
          for (int i = 0; i < 2; ++i) ok[i] = ok4(v[i], tag);
          if (__all(ok[0] & ok[1])) break;
          __builtin_amdgcn_s_sleep(2);
        }
        f32x4 q = unpack4(v[0]) + unpack4(v[1]);
#pragma unroll
        for (int i = 0; i < 4; ++i) q[i] = xrow_sum(q[i]);
        if (lane < 16) *(LAS u32x2*)(L + L_Q + a * 32 + 4 * c8 + 2 * (lane >> 3)) = u32x2{pk_bf16(q[0], q[1]), pk_bf16(q[2], q[3])};
        lds_inc(L + C_Q, lane);
      }
    } else {
      if (lane < 16) L[lane] = 0u;
      FIRST_BARRIER();
    }
  }
  __builtin_amdgcn_s_setprio(0);

  if (s == 7) {
    const int j4 = idx >> 3;
    const int rI = j4 < 2 ? 2 * xcd + j4 : 14 + 2 * xcd + j4;
    if (rI < 20) {
      const int dt = rI;
      const int r16 = lane >> 2, c4 = lane & 3;
      u32 dw[4][8];
      {
        const rsrc_t rd = mkrs(wd_ + (size_t)128 * wave * D + dt * 32, 127 * D * 2 + 64);
        const int doff = 8 * r16 * (D * 2) + c4 * 16;
        wait_until(tstart + TG_D);
        u32x4 t[8];
#pragma unroll
        for (int i = 0; i < 8; ++i) { t[i] = ld_nt(rd, doff + i * (D * 2)); SCHED(); }
#pragma unroll
        for (int k = 0; k < 4; ++k)
#pragma unroll
          for (int d = 0; d < 4; ++d) {
            dw[k][2 * d] = __builtin_amdgcn_perm(t[2 * k + 1][d], t[2 * k][d], 0x05040100u);
            dw[k][2 * d + 1] = __builtin_amdgcn_perm(t[2 * k + 1][d], t[2 * k][d], 0x07060302u);
          }
#pragma unroll
        for (int k = 0; k < 4; ++k)
#pragma unroll
          for (int c = 0; c < 8; ++c) PIN(dw[k][c]);
      }
      float ac[8];
#pragma unroll
      for (int c = 0; c < 8; ++c) { ac[c] = 0.f; PIN(ac[c]); }
      int wadr = L_T + (wave * 4 + ((lane >> 2) & 3)) * 32 + c4 * 8 + (lane >> 5) * 4 + ((lane >> 4) & 1) * 2;
      int radr = L_T + r8 * 192 + c8 * 4;
      int oadr = dt * 64 + c8 * 8;
      PIN(wadr); PIN(radr); PIN(oadr);
      const rsrc_t ro = mkrs(out_, D * 2);
      u32x2 gv, uv;
      u32x4 hv = {0u, tag, 0u, tag};
      {
        const int o0 = WS_GU + ((rI >> 1) & 3) * GU_REP + (64 * wave + lane) * 8;
        const int oh = WS_HS + xcd * HS_REP + (dt * 16 + c8 * 2) * 8;
#pragma nounroll
        for (u32 sp = 0; sp < SPIN_CAP; ++sp) {
          CBAR();
          gv = ld_sc1_64(rs, o0); uv = ld_sc1_64(rs, o0 + 6144);
          if (wave == 0) hv = ld_sc1(rs, oh);
          if (__all(((gv[1] ^ tag) | (uv[1] ^ tag) | (hv[1] ^ tag) | (hv[3] ^ tag)) == 0u)) break;
        }
      }
      auto glu = [](float g, float u) -> float { return rbf(__builtin_amdgcn_rcpf(1.0f + __builtin_amdgcn_exp2f(-g * LOG2E))) * u; };
      const u32 ha = pk_bf16(glu(lo_f(gv[0]), lo_f(uv[0])), glu(hi_f(gv[0]), hi_f(uv[0])));
      const u32 hh[4] = {dppu<0x00>(ha), dppu<0x55>(ha), dppu<0xAA>(ha), dppu<0xFF>(ha)};
#pragma unroll
      for (int k = 0; k < 4; ++k)
#pragma unroll
        for (int c = 0; c < 8; ++c) ac[c] = dot2(dw[k][c], hh[k], ac[c]);
      float b4[4];
#pragma unroll
      for (int c = 0; c < 4; ++c) b4[c] = tr32(ac[c], ac[c + 4]);
      *(LAS f32x2*)(L + wadr) = f32x2{tr16(b4[0], b4[2]), tr16(b4[1], b4[3])};
      WG_BARRIER();
      if (wave == 0) {
        f32x4 p[6];
#pragma unroll
        for (int k = 0; k < 6; ++k) p[k] = *(const LAS f32x4*)(L + radr + k * 32);
        const f32x4 t = sum_r8(((p[0] + p[1]) + (p[2] + p[3])) + (p[4] + p[5]));
        const u32 d0 = pk_bf16(t[0], t[1]), d1 = pk_bf16(t[2], t[3]);
        const u32x2 ov = {pk_bf16(lo_f(hv[0]) + lo_f(d0), hi_f(hv[0]) + hi_f(d0)), pk_bf16(lo_f(hv[2]) + lo_f(d1), hi_f(hv[2]) + hi_f(d1))};
        if (lane < 8) __builtin_amdgcn_raw_buffer_store_b64(ov, ro, oadr, 0, 16);
        if (rI == 0 && lane == 0) __builtin_amdgcn_raw_buffer_store_b32(tag, rs, WS_EPOCH, 0, 16);
      }
    } else if (rI < 30 && wave < 2) {
      const int ru = (rI - 20) * 2 + wave;
      const u32x2 xp = *(const GAS u32x2*)((const GAS char*)x_ + (ru * 32 + c8 * 4) * 2);
      f32x4 xf = {lo_f(xp[0]), hi_f(xp[0]), lo_f(xp[1]), hi_f(xp[1])};
      int sadr = WS_HS + r8 * HS_REP + (ru * 16 + c8 * 2) * 8;
      PIN(xf); PIN(sadr);
      const int gb = WS_YP + (r8 * D + ru * 32 + c8 * 4) * 4;
      u32x4 v[4];
      bool ok[4] = {false, false, false, false};
      wait_until(tstart + TG_R);
#pragma nounroll
      for (u32 sp = 0; sp < SPIN_CAP; ++sp) {
        CBAR();
#pragma unroll
        for (int i = 0; i < 4; ++i) if (!ok[i]) v[i] = ld_sc1(rs, gb + i * (8 * D * 4));
#pragma unroll
        for (int i = 0; i < 4; ++i) ok[i] = ok4(v[i], tag);
        if (__all(ok[0] & ok[1] & ok[2] & ok[3])) break;
      }
      const f32x4 y = sum_r8((unpack4(v[0]) + unpack4(v[1])) + (unpack4(v[2]) + unpack4(v[3])));
      const u32 a0 = pk_bf16(y[0], y[1]), a1 = pk_bf16(y[2], y[3]);
      st_sc1(rs, sadr, u32x4{pk_bf16(xf[0] + lo_f(a0), xf[1] + hi_f(a0)), tag, pk_bf16(xf[2] + lo_f(a1), xf[3] + hi_f(a1)), tag});
    }
    return;
  }

  const int tw = wave < 4 ? wave : wave - 4;
  if (s < 5) {
    lds_wait_ge(LV + C_VISS, 4);
    const int g4 = lane >> 4, z = lane & 15;
    const rsrc_t ro = mkrs(wo_ + (size_t)(h * DH + 24 * tw) * D + s * 128, 23 * D * 2 + 256);
    const int ooff = 6 * g4 * (D * 2) + z * 16;
    u32x4 wr[6];
    wait_until(tstart + TG_WO);
    const bool paced = __builtin_amdgcn_s_memrealtime() < tstart + TL_WO;
#pragma unroll
    for (int k = 0; k < 6; ++k) { if (k >= WOWIN && paced) vmw<WOWIN - 1>(); wr[k] = ld_nt(ro, ooff + k * (D * 2)); SCHED(); }
    u32x4 mA, mB;
#pragma unroll
    for (int d = 0; d < 4; ++d) { mA[d] = z < 8 ? msk[d] : 0u; mB[d] = z < 8 ? 0u : msk[d]; }
    int xw = L_X + tw * 288 + r8 * 36 + c8 * 4, xm = L_X + tw * 288 + c8 * 36 + 24, xo = L_X + tw * 288 + c8 * 36 + 3 * r8;
    int wadr = L_T + (tw * 2 + (g4 >> 1)) * 128 + z * 8 + (g4 & 1) * 4;
    int radr = L_T + r8 * 256 + tw * 32 + c8 * 4;
    int sadr = WS_YP + (h * D + s * 128 + tw * 32 + c8 * 4) * 4;
    PIN(mA); PIN(mB); PIN(xw); PIN(xm); PIN(xo); PIN(wadr); PIN(radr); PIN(sadr);
    lds_wait_ge(LV + C_PUB, 1);
    const int gb = WS_OP + (h * 8 + r8) * OP_REC + tw * 128 + c8 * 16;
    u32x4 va = {c8 == 6 ? 0xff800000u : 0u, tag & 0xffffu, 0u, tag & 0xffffu};
    bool ok = r8 == 7;
    wait_until(tstart + TG_O);
#pragma nounroll
    for (u32 sp = 0; sp < SPIN_CAP; ++sp) {
      CBAR();
      if (!ok) va = ld_sc1(rs, gb);
      ok = ok4(va, tag);
      if (__all(ok)) break;
    }
    *(LAS f32x4*)(L + xw) = unpack4(va);
    CBAR();
    const f32x2 ml = *(const LAS f32x2*)(L + xm);
    const float o0 = uf(L[xo]), o1 = uf(L[xo + 1]), o2 = uf(L[xo + 2]);
    SCHED();
    const float f = __builtin_amdgcn_exp2f((ml[0] - max8(ml[0])) * LOG2E);
    float t0 = f * o0, t1 = f * o1, t2 = f * o2, t3 = f * ml[1];
    t0 += dpp<XOR1>(t0); t1 += dpp<XOR1>(t1); t2 += dpp<XOR1>(t2); t3 += dpp<XOR1>(t3);
    t3 += dpp<XOR2>(t3); t0 += dpp<XOR2>(t0); t1 += dpp<XOR2>(t1); t2 += dpp<XOR2>(t2);
    t3 += dpp<HMIR>(t3); t0 += dpp<HMIR>(t0); t1 += dpp<HMIR>(t1); t2 += dpp<HMIR>(t2);
    const float li = __builtin_amdgcn_rcpf(t3);
    t0 *= li; t1 *= li; t2 *= li;
    const u32 v0 = pk_bf16(t0, t0), v1 = pk_bf16(t1, t1), v2 = pk_bf16(t2, t2);
    const f32x4 zero = {0.f, 0.f, 0.f, 0.f};
    f32x4 aA = mfma(sel(v0, mA), wr[0], zero), aB = mfma(sel(v0, mB), wr[3], zero);
    aA = mfma(sel(v1, mA), wr[1], aA); aB = mfma(sel(v1, mB), wr[4], aB);
    aA = mfma(sel(v2, mA), wr[2], aA);
    aA = mfma(sel(v2, mB), wr[5], aA);
    *(LAS f32x4*)(L + wadr) = aA + aB;
    WG_BARRIER();
    if (tw < 4) {
      const f32x4 t = sum_r8(*(const LAS f32x4*)(L + radr) + *(const LAS f32x4*)(L + radr + 128));
      const u32x4 pk = pack4(t, tag);
      if (lane < 8) st_sc1(rs, sadr, pk);
    }
    return;
  }

  const int fi = (idx >> 3) * 2 + (s - 5);
  if (fi < 6) {
    const int ft = xcd * 6 + fi, mat = ft & 1, tile = ft >> 1;
    lds_wait_ge(LV + C_VISS, 4);
    u32 gw[5][8];
    {
      const rsrc_t rg = mkrs((mat ? wu_ : wg_) + (size_t)80 * tw * F + tile * 64, 79 * F * 2 + 128);
      const int goff = 10 * r8 * (F * 2) + c8 * 16;
      u32x4 t[10];
      wait_until(tstart + TG_F);
#pragma unroll
      for (int i = 0; i < 10; ++i) {
        if (i == F_EARLY) { SCHED(); lds_wait_ge(LV + C_PUB, 1); SCHED(); }
        t[i] = ld_nt(rg, goff + i * (F * 2));
        SCHED();
      }
      SCHED();
#pragma unroll
      for (int i = 0; i < 5; ++i)
#pragma unroll
        for (int d = 0; d < 4; ++d) {
          gw[i][2 * d] = __builtin_amdgcn_perm(t[2 * i + 1][d], t[2 * i][d], 0x05040100u);
          gw[i][2 * d + 1] = __builtin_amdgcn_perm(t[2 * i + 1][d], t[2 * i][d], 0x07060302u);
        }
#pragma unroll
      for (int i = 0; i < 5; ++i)
#pragma unroll
        for (int c = 0; c < 8; ++c) PIN(gw[i][c]);
    }
    int wadr = L_T + (tw * 2 + ((lane >> 3) & 1)) * 64 + c8 * 8 + (lane >> 5) * 4 + ((lane >> 4) & 1) * 2;
    int radr = L_T + (lane >> 4) * 256 + (lane & 15) * 4;
    auto gemv = [&](const u32 (&hn)[5], float (&ac)[8]) {
#pragma unroll
      for (int i = 0; i < 5; ++i)
#pragma unroll
        for (int c = 0; c < 8; ++c) ac[c] = dot2(gw[i][c], hn[i], ac[c]);
      float b4[4];
#pragma unroll
      for (int c = 0; c < 4; ++c) b4[c] = tr32(ac[c], ac[c + 4]);
      *(LAS f32x2*)(L + wadr) = f32x2{tr16(b4[0], b4[2]), tr16(b4[1], b4[3])};
    };
    auto total = [&]() -> float {
      const f32x4 g = (*(const LAS f32x4*)(L + radr) + *(const LAS f32x4*)(L + radr + 64)) + (*(const LAS f32x4*)(L + radr + 128) + *(const LAS f32x4*)(L + radr + 192));
      return tr16(tr32(g[0], g[2]), tr32(g[1], g[3]));
    };
    float ac[8];
    {
      const u32 ones[5] = {0x3f803f80u, 0x3f803f80u, 0x3f803f80u, 0x3f803f80u, 0x3f803f80u};
#pragma unroll
      for (int c = 0; c < 8; ++c) ac[c] = 0.f;
      gemv(ones, ac);
    }
    WG_BARRIER();
    float cw = 0.f;
    if (tw == 0) cw = total();
    PIN(cw);
    WG_BARRIER();
#pragma unroll
    for (int c = 0; c < 8; ++c) { ac[c] = 0.f; PIN(ac[c]); }
    float e5 = c8 < 5 ? 1.0f : 0.0f;
    int sadr = WS_GU + (lane >> 4) * GU_REP + mat * 6144 + tile * 256 + (lane & 15) * 16;
    int stw = L_ST + tw, str = L_ST + c8;
    PIN(e5); PIN(sadr); PIN(wadr); PIN(radr); PIN(stw); PIN(str);
    u32x2 hv;
    {
      const int o0 = WS_HS + xcd * HS_REP + (40 * tw + 5 * r8 + (c8 < 5 ? c8 : 0)) * 8;
#pragma nounroll
      for (u32 sp = 0; sp < SPIN_CAP; ++sp) {
        CBAR();
        hv = ld_sc1_64(rs, o0);
        if (__all(hv[1] == tag)) break;
      }
    }
    u32 hn[5];
    hn[0] = __builtin_amdgcn_ds_swizzle(hv[0], 0x18 | (0 << 5));
    hn[1] = __builtin_amdgcn_ds_swizzle(hv[0], 0x18 | (1 << 5));
    hn[2] = __builtin_amdgcn_ds_swizzle(hv[0], 0x18 | (2 << 5));
    hn[3] = __builtin_amdgcn_ds_swizzle(hv[0], 0x18 | (3 << 5));
    hn[4] = __builtin_amdgcn_ds_swizzle(hv[0], 0x18 | (4 << 5));
    const float pv = lo_f(__builtin_amdgcn_readfirstlane(hv[0]));
    const float d0 = (lo_f(hv[0]) - pv) * e5, d1 = (hi_f(hv[0]) - pv) * e5;
    float s1 = sum8(d0 + d1), s2 = sum8(d0 * d0 + d1 * d1);
    s1 += dpp<ROR8>(s1); s2 += dpp<ROR8>(s2);
    float ss = tr32(s1, s2);
    ss = tr16(ss, ss);
    gemv(hn, ac);
    {
      auto sw = __builtin_amdgcn_permlane32_swap(fu(ss), fu(ss), false, false);
      const float t1 = uf(sw[0]), t2 = uf(sw[1]);
      if (lane == 0) { L[stw] = fu(pv + t1 * (1.0f / 80.0f)); L[stw + 8] = fu(t2 - t1 * t1 * (1.0f / 80.0f)); }
    }
    WG_BARRIER();
    if (tw == 0) {
      const float mw = uf(L[str]), qw = uf(L[str + 8]);
      const float G = total();
      const float mean = sum8(mw) * 0.125f;
      const float var = rbf(sum8(qw + 80.0f * (mw - mean) * (mw - mean)) * (1.0f / D));
      const float rinv = __builtin_amdgcn_rcpf(rbf(__builtin_amdgcn_sqrtf(rbf(var + 1e-5f))));
      const float g = (G - rbf(mean) * cw) * rinv;
      auto sa = __builtin_amdgcn_permlane16_swap(fu(g), fu(g), false, false);
      const u32 pr = pk_bf16(uf(sa[0]), uf(sa[1]));
      auto sb = __builtin_amdgcn_permlane32_swap(pr, pr, false, false);
      st_sc1(rs, sadr, u32x4{sb[0], tag, sb[1], tag});
    }
  }
}

extern "C" void kernel_launch(void* const* d_in, const int* in_sizes, int n_in,
                              void* d_out, int out_size, void* d_ws, size_t ws_size,
                              hipStream_t stream) {
  hipLaunchKernelGGL(fused_block, dim3(256), dim3(NTHR), 0, stream,
                     (const u16*)d_in[0], (const u16*)d_in[1], (const u16*)d_in[2], (const u16*)d_in[3], (const u16*)d_in[4],
                     (const u16*)d_in[6], (const u16*)d_in[7], (const u16*)d_in[8], (u16*)d_out, (unsigned char*)d_ws);
}
```

```cpp
#include <hip/hip_runtime.h>
#include <hip/hip_bf16.h>
#include <stdint.h>

typedef unsigned short u16;
typedef unsigned u32;
typedef unsigned long long u64;
typedef u32 u32x4 __attribute__((ext_vector_type(4)));
typedef u32 u32x2 __attribute__((ext_vector_type(2)));
typedef float f32x4 __attribute__((ext_vector_type(4)));
typedef float f32x2 __attribute__((ext_vector_type(2)));
typedef __bf16 bf16x8 __attribute__((ext_vector_type(8)));
typedef __bf16 bf16x2 __attribute__((ext_vector_type(2)));
#define GAS __attribute__((address_space(1)))
#define LAS __attribute__((address_space(3)))

constexpr int D = 640, H = 32, DH = 192, SK = 8192, W = 2048, F = 1536, HD = H * DH;
constexpr int NTHR = 768;
constexpr float LOG2E = 1.4426950408889634f;

constexpr int WIN = 8;
constexpr int WVA = 9, WVB = 7;
constexpr int KGATE = 24;
constexpr int WOWIN = 3;
constexpr int F_EARLY = 4;
constexpr int TG_QP = 395, TG_Q = 495, TG_WO = 775, TG_F = 785, TG_D = 895, TG_P = 945, TG_O = 1015, TG_R = 1155;
constexpr int LI1 = 22, LT1 = 670, LWIN = 14, TL_WO = 820;
constexpr int PF_BYTES = 15872, PF_REP = 3, PF_PER = 400;

constexpr int WS_EPOCH = 0;
constexpr int HS_REP = 2560;
constexpr int WS_HS = 4096;
constexpr int GU_REP = 12288;
constexpr int WS_GU = 32768;
constexpr int WS_YP = WS_GU + 4 * GU_REP;
constexpr int WS_QP = WS_YP + H * D * 4;
constexpr int OP_REC = 1024;
constexpr int WS_OP = WS_QP + H * 8 * DH * 4;
constexpr int WS_END = WS_OP + H * 8 * OP_REC;
constexpr u32 SPIN_CAP = 1u << 21;

constexpr int C_Q = 0, C_KISS = 1, C_VISS = 2, C_O = 3, C_P = 4, C_PUB = 5, C_PF = 8;
constexpr int L_Q = 16;
constexpr int L_P = 112;
constexpr int L_ML = L_P + 384;
constexpr int L_O = L_ML + 8;
constexpr int L_T = L_O + 8 * 192;
constexpr int L_ST = L_T + 2048;
constexpr int L_X = L_ST + 16;
constexpr int L_END = L_X + 8 * 288;

__device__ __forceinline__ u32 fu(float f) { return __float_as_uint(f); }
__device__ __forceinline__ float uf(u32 u) { return __uint_as_float(u); }
__device__ __forceinline__ float lo_f(u32 u) { return uf(u << 16); }
__device__ __forceinline__ float hi_f(u32 u) { return uf(u & 0xffff0000u); }
__device__ __forceinline__ u32 pk_bf16(float lo, float hi) {
  bf16x2 v = {(__bf16)lo, (__bf16)hi};
  return __builtin_bit_cast(u32, v);
}
__device__ __forceinline__ float rbf(float f) { return (float)(__bf16)f; }
__device__ __forceinline__ float dot2(u32 a, u32 b, float acc) {
  return __builtin_amdgcn_fdot2_f32_bf16(__builtin_bit_cast(bf16x2, a), __builtin_bit_cast(bf16x2, b), acc, false);
}
__device__ __forceinline__ f32x4 mfma(u32x4 a, u32x4 b, f32x4 c) {
  return __builtin_amdgcn_mfma_f32_16x16x32_bf16(__builtin_bit_cast(bf16x8, a), __builtin_bit_cast(bf16x8, b), c, 0, 0, 0);
}
__device__ __forceinline__ u32x4 sel(u32 v, u32x4 m) { return u32x4{v & m[0], v & m[1], v & m[2], v & m[3]}; }
template <int CTRL> __device__ __forceinline__ float dpp(float x) {
  return __builtin_bit_cast(float, __builtin_amdgcn_mov_dpp(__builtin_bit_cast(int, x), CTRL, 0xf, 0xf, true));
}
template <int CTRL> __device__ __forceinline__ u32 dppu(u32 x) { return (u32)__builtin_amdgcn_mov_dpp((int)x, CTRL, 0xf, 0xf, true); }
constexpr int XOR1 = 0xB1, XOR2 = 0x4E, HMIR = 0x141, ROR8 = 0x128;
__device__ __forceinline__ float sum8(float x) { x += dpp<XOR1>(x); x += dpp<XOR2>(x); x += dpp<HMIR>(x); return x; }
__device__ __forceinline__ float max8(float x) {
  float r;
  asm volatile("s_nop 1\n\tv_max_f32_dpp %0, %1, %1 quad_perm:[1,0,3,2] row_mask:0xf bank_mask:0xf\n\t"
               "s_nop 1\n\tv_max_f32_dpp %0, %0, %0 quad_perm:[2,3,0,1] row_mask:0xf bank_mask:0xf\n\t"
               "s_nop 1\n\tv_max_f32_dpp %0, %0, %0 row_half_mirror row_mask:0xf bank_mask:0xf" : "=&v"(r) : "v"(x));
  return r;
}
__device__ __forceinline__ float max16(float x) {
  float r;
  asm volatile("s_nop 1\n\tv_max_f32_dpp %0, %1, %1 quad_perm:[1,0,3,2] row_mask:0xf bank_mask:0xf\n\t"
               "s_nop 1\n\tv_max_f32_dpp %0, %0, %0 quad_perm:[2,3,0,1] row_mask:0xf bank_mask:0xf\n\t"
               "s_nop 1\n\tv_max_f32_dpp %0, %0, %0 row_half_mirror row_mask:0xf bank_mask:0xf\n\t"
               "s_nop 1\n\tv_max_f32_dpp %0, %0, %0 row_ror:8 row_mask:0xf bank_mask:0xf\n\ts_nop 1" : "=&v"(r) : "v"(x));
  return r;
}
__device__ __forceinline__ float tr32(float a, float b) {
  auto t = __builtin_amdgcn_permlane32_swap(fu(a), fu(b), false, false);
  return uf(t[0]) + uf(t[1]);
}
__device__ __forceinline__ float tr16(float a, float b) {
  auto t = __builtin_amdgcn_permlane16_swap(fu(a), fu(b), false, false);
  return uf(t[0]) + uf(t[1]);
}
__device__ __forceinline__ float xrow_sum(float x) { x = tr16(x, x); return tr32(x, x); }
__device__ __forceinline__ float xrow_max(float x) {
  auto s = __builtin_amdgcn_permlane16_swap(fu(x), fu(x), false, false);
  x = fmaxf(uf(s[0]), uf(s[1]));
  auto t = __builtin_amdgcn_permlane32_swap(fu(x), fu(x), false, false);
  return fmaxf(uf(t[0]), uf(t[1]));
}
__device__ __forceinline__ float sum64(float x) { x = sum8(x); x += dpp<ROR8>(x); return xrow_sum(x); }
__device__ __forceinline__ f32x4 sum_r8(f32x4 t) {
  float w = tr16(tr32(t[0], t[1]), tr32(t[2], t[3]));
  w += dpp<ROR8>(w);
  auto sa = __builtin_amdgcn_permlane16_swap(fu(w), fu(w), false, false);
  auto se = __builtin_amdgcn_permlane32_swap(sa[0], sa[0], false, false);
  auto so = __builtin_amdgcn_permlane32_swap(sa[1], sa[1], false, false);
  return f32x4{uf(se[0]), uf(se[1]), uf(so[0]), uf(so[1])};
}

typedef __amdgpu_buffer_rsrc_t rsrc_t;
__device__ __forceinline__ rsrc_t mkrs(const void* p, int bytes) { return __builtin_amdgcn_make_buffer_rsrc((void*)p, 0, bytes, 0x00020000); }
__device__ __forceinline__ u32x4 ld_nt(rsrc_t rs, int off) { return __builtin_amdgcn_raw_buffer_load_b128(rs, off, 0, 2); }
__device__ __forceinline__ u32x4 ld_sc1(rsrc_t rs, int off) { return __builtin_amdgcn_raw_buffer_load_b128(rs, off, 0, 16); }
__device__ __forceinline__ u32x2 ld_sc1_64(rsrc_t rs, int off) { return __builtin_amdgcn_raw_buffer_load_b64(rs, off, 0, 16); }
__device__ __forceinline__ void st_sc1(rsrc_t rs, int off, u32x4 v) { __builtin_amdgcn_raw_buffer_store_b128(v, rs, off, 0, 16); }
template <int N0, int N1, int N2> __device__ __forceinline__ void vmw3(int mode) {
  asm volatile("s_cmp_eq_u32 %0, 2\n\ts_cbranch_scc1 1f\n\ts_waitcnt vmcnt(%2)\n\ts_cmp_eq_u32 %0, 1\n\ts_cbranch_scc1 1f\n\t"
               "s_waitcnt vmcnt(%1)\n1:\n\ts_waitcnt vmcnt(%3)" :: "s"(mode), "i"(N0), "i"(N1), "i"(N2) : "memory", "scc");
}
template <int N> __device__ __forceinline__ void vmw() { asm volatile("s_waitcnt vmcnt(%0)" :: "i"(N) : "memory"); }

__device__ __forceinline__ u32x4 pack4(f32x4 v, u32 tag) {
  const u32 a = fu(v[0]) + 0x80u, b = fu(v[1]) + 0x80u, c = fu(v[2]) + 0x80u, d = fu(v[3]) + 0x80u;
  return u32x4{__builtin_amdgcn_perm(a, b, 0x07060503u), __builtin_amdgcn_perm(b, tag, 0x06050100u),
               __builtin_amdgcn_perm(c, d, 0x07060503u), __builtin_amdgcn_perm(d, tag, 0x06050100u)};
}
__device__ __forceinline__ f32x4 unpack4(u32x4 w) {
  return f32x4{uf(w[0] & 0xffffff00u), uf(__builtin_amdgcn_perm(w[0], w[1], 0x0403020cu)),
               uf(w[2] & 0xffffff00u), uf(__builtin_amdgcn_perm(w[2], w[3], 0x0403020cu))};
}
__device__ __forceinline__ bool ok4(u32x4 w, u32 tag) { return (((w[1] ^ tag) | (w[3] ^ tag)) & 0xffffu) == 0u; }

#define CBAR() asm volatile("" ::: "memory")
#define PIN(x) asm volatile("" : "+v"(x))
#define LGKM0() asm volatile("s_waitcnt lgkmcnt(0)" ::: "memory")
#define WG_BARRIER() asm volatile("s_waitcnt lgkmcnt(0)\n\ts_barrier" ::: "memory")
#define SCHED() __builtin_amdgcn_sched_barrier(0)

__device__ __forceinline__ void lds_wait_ge(volatile LAS u32* w, u32 n) {
#pragma nounroll
  for (u32 sp = 0; sp < SPIN_CAP; ++sp) {
    if (*w >= n) break;
    __builtin_amdgcn_s_sleep(1);
  }
  CBAR();
}
__device__ __forceinline__ void wait_until(u64 t) {
#pragma nounroll
  for (u32 sp = 0; sp < 4096; ++sp) {
    if (__builtin_amdgcn_s_memrealtime() >= t) break;
    __builtin_amdgcn_s_sleep(1);
  }
  CBAR();
}
__device__ __forceinline__ void lds_inc(LAS u32* w, int lane) {
  CBAR();
  if (lane == 0) __hip_atomic_fetch_add(w, 1u, __ATOMIC_RELAXED, __HIP_MEMORY_SCOPE_WORKGROUP);
  CBAR();
}

__global__ void __launch_bounds__(NTHR, 3)
fused_block(const u16* __restrict__ x_, const u16* __restrict__ kc_, const u16* __restrict__ vc_, const u16* __restrict__ wq_,
            const u16* __restrict__ wo_, const u16* __restrict__ wg_, const u16* __restrict__ wu_, const u16* __restrict__ wd_,
            u16* __restrict__ out_, unsigned char* ws_) {
  u64 pc0; asm volatile("s_getpc_b64 %0" : "=s"(pc0));
  asm volatile("" :: "s"(x_), "s"(kc_), "s"(vc_), "s"(wq_), "s"(wo_), "s"(wg_), "s"(wu_), "s"(wd_), "s"(out_), "s"(ws_));
  u32 epw;
  asm volatile("s_load_dword %0, %1, 0x0\n\ts_nop 0" : "=&s"(epw) : "s"(ws_) : "memory");
  __shared__ __attribute__((aligned(16))) u32 smem[L_END];
  LAS u32* L = (LAS u32*)smem;
  volatile LAS u32* LV = (volatile LAS u32*)smem;
  const int tid = threadIdx.x, lane = tid & 63, wave = __builtin_amdgcn_readfirstlane(tid >> 6);
  const int b = blockIdx.x;
  const int xcd = b & 7, idx = b >> 3;
  const int h = xcd * 4 + (idx >> 3), s = idx & 7;
  const rsrc_t rs = mkrs(ws_, WS_END);
  const int r8 = lane >> 3, c8 = lane & 7;
  const u64 tstart = __builtin_amdgcn_s_memrealtime();
  u32x4 msk;
#pragma unroll
  for (int d = 0; d < 4; ++d) msk[d] = (d == (c8 >> 1)) ? ((c8 & 1) ? 0xffff0000u : 0x0000ffffu) : 0u;
  u32 tag;
#define FIRST_BARRIER() do { asm volatile("s_waitcnt lgkmcnt(0)\n\ts_barrier\n\ts_add_u32 %0, %1, 1" : "=s"(tag) : "s"(epw) : "memory", "scc"); } while (0)
  const int g0 = 36 * s + (s > 3 ? s - 3 : 0), ng = s >= 3 ? 37 : 36;
  const size_t kvoff = ((size_t)h * SK + (SK - W) + g0 * 8) * DH;

  if (wave < 8 && s == 7) {
    if (wave == 4 && idx >= 16) {
      const rsrc_t rc = mkrs((const void*)(pc0 & ~127ull), PF_BYTES);
      const int off = ((3 - (idx >> 3)) * 64 + lane) * 128;
      u32 t0 = __builtin_amdgcn_raw_buffer_load_b32(rc, off, 0, 2);
      FIRST_BARRIER();
      asm volatile("" :: "v"(t0));
#pragma nounroll
      for (int r = 1; r < PF_REP; ++r) {
        wait_until(tstart + r * PF_PER);
        u32 t1 = __builtin_amdgcn_raw_buffer_load_b32(rc, off, 0, 2);
        asm volatile("" :: "v"(t1));
      }
    } else {
      if (wave == 5) {
        const size_t win = ((size_t)h * SK + (SK - W)) * DH * 2;
        const int q4 = idx >> 3;
        const GAS char* p;
        if (lane < 13) p = (const GAS char*)vc_ + win + (lane < 12 ? lane * 65536 : W * DH * 2 - 64);
        else if (lane < 26) p = (const GAS char*)kc_ + win + (lane < 25 ? (lane - 13) * 65536 : W * DH * 2 - 64);
        else if (lane < 31) p = (const GAS char*)wo_ + (size_t)h * DH * D * 2 + (lane < 30 ? (lane - 26) * 65536 : DH * D * 2 - 64);
        else if (lane < 39) p = (const GAS char*)wg_ + q4 * 491520 + (lane - 31) * 61440;
        else if (lane < 47) p = (const GAS char*)wu_ + q4 * 491520 + (lane - 39) * 61440;
        else if (lane < 55) p = (const GAS char*)wd_ + q4 * 491520 + (lane - 47) * 61440;
        else p = (const GAS char*)out_;
        u32 t0 = __builtin_nontemporal_load((const GAS u32*)p);
        FIRST_BARRIER();
        asm volatile("" :: "v"(t0));
      } else {
        FIRST_BARRIER();
      }
    }
  } else if (wave < 4) {
    __builtin_amdgcn_s_setprio(2);
    const rsrc_t rk = mkrs(kc_ + kvoff, ng * 3072);
    const int loff = wave * 3072 + r8 * 384 + c8 * 16;
    u32x4 kr[30];
#pragma unroll
    for (int i = 0; i < 30; ++i) {
      if (i == WIN) FIRST_BARRIER();
      if (i >= WIN) vmw<WIN - 1>();
      if (i == KGATE) lds_inc(L + C_KISS, lane);
      kr[i] = ld_nt(rk, loff + (i / 3) * 12288 + (i % 3) * 128);
      SCHED();
    }
    float dd[10];
    const bool sbok = c8 == 0 || (c8 == 1 && 36 + wave < ng);
    int padr = L_P + wave * 96 + r8 * 12 + c8, padr2 = c8 < 2 ? padr + 8 : L_T + lane;
    PIN(padr); PIN(padr2);
#pragma unroll
    for (int g = 0; g < 10; ++g) { dd[g] = 0.f; PIN(dd[g]); }
    lds_wait_ge(LV + C_Q, 3);
    u32x4 qp[3];
#pragma unroll
    for (int t = 0; t < 3; ++t) qp[t] = *(const LAS u32x4*)(L + L_Q + t * 32 + c8 * 4);
#pragma unroll
    for (int e = 0; e < 12; ++e) {
#pragma unroll
      for (int g = 0; g < 10; ++g) dd[g] = dot2(kr[g * 3 + e / 4][e % 4], qp[e / 4][e % 4], dd[g]);
      SCHED();
    }
    const bool b0 = c8 & 1, b1 = c8 & 2, b2 = c8 & 4;
    float e4[4], e2[2];
#pragma unroll
    for (int i = 0; i < 4; ++i) { const float keep = b2 ? dd[i + 4] : dd[i], give = b2 ? dd[i] : dd[i + 4]; e4[i] = keep + dpp<HMIR>(give); }
#pragma unroll
    for (int i = 0; i < 2; ++i) { const float keep = b1 ? e4[i + 2] : e4[i], give = b1 ? e4[i] : e4[i + 2]; e2[i] = keep + dpp<XOR2>(give); }
    float sa = (b0 ? e2[1] : e2[0]) + dpp<XOR1>(b0 ? e2[0] : e2[1]);
    float sb = (b0 ? dd[9] : dd[8]) + dpp<XOR1>(b0 ? dd[8] : dd[9]);
    sb += dpp<XOR2>(sb);
    sb += dpp<0x1B>(dpp<HMIR>(sb));
    sa = rbf(rbf(sa) * 0.07216878364870323f);
    sb = rbf(rbf(sb) * 0.07216878364870323f);
    sb = sbok ? sb : -INFINITY;
    const float m = xrow_max(max16(fmaxf(sa, sb)));
    const float pa = __builtin_amdgcn_exp2f((sa - m) * LOG2E), pb = __builtin_amdgcn_exp2f((sb - m) * LOG2E);
    L[padr] = pk_bf16(pa, pa);
    L[padr2] = pk_bf16(pb, pb);
    CBAR();
    if (lane == 0) LV[C_PF + wave] = 1u;
    const float l = sum64(pa + pb);
    if (lane == 0) { L[L_ML + wave] = fu(m); L[L_ML + 4 + wave] = fu(l); }
    lds_inc(L + C_P, lane);
  } else if (wave < 8) {
    __builtin_amdgcn_s_setprio(1);
    const int w = wave - 4;
    const rsrc_t rv = mkrs(vc_ + kvoff, ng * 3072);
    const int loff = w * 3072 + r8 * 384 + c8 * 16;
    FIRST_BARRIER();
    lds_wait_ge(LV + C_KISS, 4);
    u32x4 vr[30];
    int mode = __builtin_amdgcn_readfirstlane(s < 5 ? 1 : 0);
#pragma unroll
    for (int i = 0; i < 30; ++i) {
      const int j = (i + 27) % 30;
      if (i == LI1) mode = __builtin_amdgcn_readfirstlane(__builtin_amdgcn_s_memrealtime() > tstart + LT1 ? 2 : mode);
      if (i >= WVB) vmw3<WVB - 1, WVA - 1, LWIN - 1>(mode);
      if (i == 29) lds_inc(L + C_VISS, lane);
      vr[j] = ld_nt(rv, loff + (j / 3) * 12288 + (j % 3) * 128);
      SCHED();
    }
    int wadr = ((lane >> 5) == ((lane >> 3) & 1) ? L_O + (w * 2 + (lane >> 5)) * 192 : L_T + 64 + w * 192) + c8 * 8 + ((lane >> 4) & 1) * 4;
    int radr = L_O + 24 * r8 + 4 * (c8 < 6 ? c8 : 0);
    int sadr = WS_OP + (h * 8 + s) * OP_REC + lane * 16;
    u32 tadr = (u32)(uintptr_t)(L + C_O), one = 1u;
    PIN(wadr); PIN(radr); PIN(sadr); PIN(tadr); PIN(one);
    lds_wait_ge(LV + C_PF + w, 1);
    const LAS u32* pr = L + L_P + w * 96 + r8 * 12;
    const u32x4 p0 = *(const LAS u32x4*)(pr), p1 = *(const LAS u32x4*)(pr + 4);
    const u32x2 p2 = *(const LAS u32x2*)(pr + 8);
    float M, Ls, fw;
    f32x4 acc[2][3];
#pragma unroll
    for (int gg = 0; gg < 10; ++gg) {
      const int g = (gg + 9) % 10;
      if (gg == 5) {
        SCHED();
        lds_wait_ge(LV + C_P, 4);
        const f32x4 mm = *(const LAS f32x4*)(L + L_ML), ll = *(const LAS f32x4*)(L + L_ML + 4);
        M = fmaxf(fmaxf(mm[0], mm[1]), fmaxf(mm[2], mm[3]));
        f32x4 ff;
#pragma unroll
        for (int k = 0; k < 4; ++k) ff[k] = __builtin_amdgcn_exp2f((mm[k] - M) * LOG2E);
        Ls = (ff[0] * ll[0] + ff[1] * ll[1]) + (ff[2] * ll[2] + ff[3] * ll[3]);
        fw = w == 0 ? ff[0] : (w == 1 ? ff[1] : (w == 2 ? ff[2] : ff[3]));
        PIN(M); PIN(Ls); PIN(fw);
      }
      const u32x4 A = sel(g < 4 ? p0[g & 3] : (g < 8 ? p1[g & 3] : p2[g & 1]), msk);
#pragma unroll
      for (int t = 0; t < 3; ++t) {
        if (gg == 9) acc[0][t] = mfma(A, vr[g * 3 + t], acc[0][t] + acc[1][t]);
        else acc[gg & 1][t] = mfma(A, vr[g * 3 + t], gg < 2 ? f32x4{0.f, 0.f, 0.f, 0.f} : acc[gg & 1][t]);
      }
    }
#pragma unroll
    for (int t = 0; t < 3; ++t) *(LAS f32x4*)(L + wadr + t * 64) = acc[0][t] * fw;
    u32 old;
    asm volatile("s_mov_b64 exec, 1\n\tds_add_rtn_u32 %0, %1, %2\n\ts_mov_b64 exec, -1\n\ts_waitcnt lgkmcnt(0)" : "=&v"(old) : "v"(tadr), "v"(one) : "memory");
    if (__builtin_amdgcn_readfirstlane(old) == 3) {
      const u64 tnow = __builtin_amdgcn_s_memrealtime();
      f32x4 pt[8];
#pragma unroll
      for (int k = 0; k < 8; ++k) pt[k] = *(const LAS f32x4*)(L + radr + k * 192);
      SCHED();
      f32x4 o = ((pt[0] + pt[1]) + (pt[2] + pt[3])) + ((pt[4] + pt[5]) + (pt[6] + pt[7]));
      if (c8 == 6) { o[0] = M; o[1] = Ls; }
      u32x4 pk = pack4(o, tag);
      PIN(pk);
      if (tnow < tstart + TG_P) wait_until(tstart + TG_P);
      st_sc1(rs, sadr, pk);
      if (lane == 0) LV[C_PUB] = 1u;
    }
    return;
  } else {
    const int a = wave - 8;
    __builtin_amdgcn_s_setprio(3);
    if (a < 3) {
      const GAS char* xb = (const GAS char*)x_;
      const u32x4 xa = *(const GAS u32x4*)(xb + lane * 16);
      const u32 xc = *(const GAS u32*)(xb + 1024 + lane * 4);
      const GAS u32* xr = (const GAS u32*)(xb + 160 * s + 20 * r8);
      u32 xo[5];
#pragma unroll
      for (int i = 0; i < 5; ++i) xo[i] = xr[i];
      const rsrc_t rq = mkrs(wq_ + (size_t)80 * s * HD + h * DH + a * 64, 79 * HD * 2 + 128);
      const int qoff = 10 * r8 * (HD * 2) + c8 * 16;
      u32x4 wr[10];
#pragma unroll
      for (int g = 0; g < 10; ++g) wr[g] = ld_nt(rq, qoff + g * (HD * 2));
      SCHED();
      FIRST_BARRIER();
      SCHED();
      const float xv[10] = {lo_f(xa[0]), hi_f(xa[0]), lo_f(xa[1]), hi_f(xa[1]), lo_f(xa[2]), hi_f(xa[2]), lo_f(xa[3]), hi_f(xa[3]), lo_f(xc), hi_f(xc)};
      float sm = 0.f;
#pragma unroll
      for (int i = 0; i < 10; ++i) sm += xv[i];
      const float mean = sum64(sm) * (1.0f / D);
      float sq = 0.f;
#pragma unroll
      for (int i = 0; i < 10; ++i) { const float dd = xv[i] - mean; sq += dd * dd; }
      const float var = rbf(sum64(sq) * (1.0f / D));
      const float mb = rbf(mean);
      const float rinv = 1.0f / rbf(__builtin_sqrtf(rbf(var + 1e-5f)));
      f32x4 acc = {0.f, 0.f, 0.f, 0.f};
#pragma unroll
      for (int g = 0; g < 10; ++g) {
        const float xg = (g & 1) ? hi_f(xo[g >> 1]) : lo_f(xo[g >> 1]);
        const float xn = rbf(xg - mb) * rinv;
        acc = mfma(sel(pk_bf16(xn, xn), msk), wr[g], acc);
      }
#pragma unroll
      for (int i = 0; i < 4; ++i) {
        const float up = dpp<ROR8>(acc[i]);
        auto t = __builtin_amdgcn_permlane32_swap(fu(up), fu(up), false, false);
        acc[i] += uf(t[1]);
      }
      u32x4 pk = pack4(acc, tag);
      PIN(pk);
      wait_until(tstart + TG_QP);
      if (lane < 32 && (lane & 8) == 0) st_sc1(rs, WS_QP + (h * 8 + s) * (DH * 4) + a * 256 + (c8 + 8 * (lane >> 4)) * 16, pk);
      if (s < 7) {
        const int gb = WS_QP + (h * 8 + (lane >> 4)) * (DH * 4) + a * 256 + (lane & 15) * 16;
        u32x4 v[2];
        bool ok[2] = {false, false};
        wait_until(tstart + TG_Q);
#pragma nounroll
        for (u32 sp = 0; sp < SPIN_CAP; ++sp) {
          CBAR();
#pragma unroll
          for (int i = 0; i < 2; ++i) if (!ok[i]) v[i] = ld_sc1(rs, gb + i * (4 * DH * 4));
#pragma unroll
          for (int i = 0; i < 2; ++i) ok[i] = ok4(v[i], tag);
          if (__all(ok[0] & ok[1])) break;
          __builtin_amdgcn_s_sleep(2);
        }
        f32x4 q = unpack4(v[0]) + unpack4(v[1]);
#pragma unroll
        for (int i = 0; i < 4; ++i) q[i] = xrow_sum(q[i]);
        if (lane < 16) *(LAS u32x2*)(L + L_Q + a * 32 + 4 * c8 + 2 * (lane >> 3)) = u32x2{pk_bf16(q[0], q[1]), pk_bf16(q[2], q[3])};
        lds_inc(L + C_Q, lane);
      }
    } else {
      if (lane < 16) L[lane] = 0u;
      FIRST_BARRIER();
    }
  }
  __builtin_amdgcn_s_setprio(0);

  if (s == 7) {
    const int j4 = idx >> 3;
    const int rI = j4 < 2 ? 2 * xcd + j4 : 14 + 2 * xcd + j4;
    if (rI < 20) {
      const int dt = rI;
      const int r16 = lane >> 2, c4 = lane & 3;
      u32 dw[4][8];
      {
        const rsrc_t rd = mkrs(wd_ + (size_t)128 * wave * D + dt * 32, 127 * D * 2 + 64);
        const int doff = 8 * r16 * (D * 2) + c4 * 16;
        wait_until(tstart + TG_D);
        u32x4 t[8];
#pragma unroll
        for (int i = 0; i < 8; ++i) { t[i] = ld_nt(rd, doff + i * (D * 2)); SCHED(); }
#pragma unroll
        for (int k = 0; k < 4; ++k)
#pragma unroll
          for (int d = 0; d < 4; ++d) {
            dw[k][2 * d] = __builtin_amdgcn_perm(t[2 * k + 1][d], t[2 * k][d], 0x05040100u);
            dw[k][2 * d + 1] = __builtin_amdgcn_perm(t[2 * k + 1][d], t[2 * k][d], 0x07060302u);
          }
#pragma unroll
        for (int k = 0; k < 4; ++k)
#pragma unroll
          for (int c = 0; c < 8; ++c) PIN(dw[k][c]);
      }
      float ac[8];
#pragma unroll
      for (int c = 0; c < 8; ++c) { ac[c] = 0.f; PIN(ac[c]); }
      int wadr = L_T + (wave * 4 + ((lane >> 2) & 3)) * 32 + c4 * 8 + (lane >> 5) * 4 + ((lane >> 4) & 1) * 2;
      int radr = L_T + r8 * 192 + c8 * 4;
      int oadr = dt * 64 + c8 * 8;
      PIN(wadr); PIN(radr); PIN(oadr);
      const rsrc_t ro = mkrs(out_, D * 2);
      u32x2 gv, uv;
      u32x4 hv = {0u, tag, 0u, tag};
      {
        const int o0 = WS_GU + ((rI >> 1) & 3) * GU_REP + (64 * wave + lane) * 8;
        const int oh = WS_HS + xcd * HS_REP + (dt * 16 + c8 * 2) * 8;
#pragma nounroll
        for (u32 sp = 0; sp < SPIN_CAP; ++sp) {
          CBAR();
          gv = ld_sc1_64(rs, o0); uv = ld_sc1_64(rs, o0 + 6144);
          if (wave == 0) hv = ld_sc1(rs, oh);
          if (__all(((gv[1] ^ tag) | (uv[1] ^ tag) | (hv[1] ^ tag) | (hv[3] ^ tag)) == 0u)) break;
        }
      }
      auto glu = [](float g, float u) -> float { return rbf(__builtin_amdgcn_rcpf(1.0f + __builtin_amdgcn_exp2f(-g * LOG2E))) * u; };
      const u32 ha = pk_bf16(glu(lo_f(gv[0]), lo_f(uv[0])), glu(hi_f(gv[0]), hi_f(uv[0])));
      const u32 hh[4] = {dppu<0x00>(ha), dppu<0x55>(ha), dppu<0xAA>(ha), dppu<0xFF>(ha)};
#pragma unroll
      for (int k = 0; k < 4; ++k)
#pragma unroll
        for (int c = 0; c < 8; ++c) ac[c] = dot2(dw[k][c], hh[k], ac[c]);
      float b4[4];
#pragma unroll
      for (int c = 0; c < 4; ++c) b4[c] = tr32(ac[c], ac[c + 4]);
      *(LAS f32x2*)(L + wadr) = f32x2{tr16(b4[0], b4[2]), tr16(b4[1], b4[3])};
      WG_BARRIER();
      if (wave == 0) {
        f32x4 p[6];
#pragma unroll
        for (int k = 0; k < 6; ++k) p[k] = *(const LAS f32x4*)(L + radr + k * 32);
        const f32x4 t = sum_r8(((p[0] + p[1]) + (p[2] + p[3])) + (p[4] + p[5]));
        const u32 d0 = pk_bf16(t[0], t[1]), d1 = pk_bf16(t[2], t[3]);
        const u32x2 ov = {pk_bf16(lo_f(hv[0]) + lo_f(d0), hi_f(hv[0]) + hi_f(d0)), pk_bf16(lo_f(hv[2]) + lo_f(d1), hi_f(hv[2]) + hi_f(d1))};
        if (lane < 8) __builtin_amdgcn_raw_buffer_store_b64(ov, ro, oadr, 0, 16);
        if (rI == 0 && lane == 0) __builtin_amdgcn_raw_buffer_store_b32(tag, rs, WS_EPOCH, 0, 16);
      }
    } else if (rI < 30 && wave < 2) {
      const int ru = (rI - 20) * 2 + wave;
      const u32x2 xp = *(const GAS u32x2*)((const GAS char*)x_ + (ru * 32 + c8 * 4) * 2);
      f32x4 xf = {lo_f(xp[0]), hi_f(xp[0]), lo_f(xp[1]), hi_f(xp[1])};
      int sadr = WS_HS + r8 * HS_REP + (ru * 16 + c8 * 2) * 8;
      PIN(xf); PIN(sadr);
      const int gb = WS_YP + (r8 * D + ru * 32 + c8 * 4) * 4;
      u32x4 v[4];
      bool ok[4] = {false, false, false, false};
      wait_until(tstart + TG_R);
#pragma nounroll
      for (u32 sp = 0; sp < SPIN_CAP; ++sp) {
        CBAR();
#pragma unroll
        for (int i = 0; i < 4; ++i) if (!ok[i]) v[i] = ld_sc1(rs, gb + i * (8 * D * 4));
#pragma unroll
        for (int i = 0; i < 4; ++i) ok[i] = ok4(v[i], tag);
        if (__all(ok[0] & ok[1] & ok[2] & ok[3])) break;
      }
      const f32x4 y = sum_r8((unpack4(v[0]) + unpack4(v[1])) + (unpack4(v[2]) + unpack4(v[3])));
      const u32 a0 = pk_bf16(y[0], y[1]), a1 = pk_bf16(y[2], y[3]);
      st_sc1(rs, sadr, u32x4{pk_bf16(xf[0] + lo_f(a0), xf[1] + hi_f(a0)), tag, pk_bf16(xf[2] + lo_f(a1), xf[3] + hi_f(a1)), tag});
    }
    return;
  }

  const int tw = wave < 4 ? wave : wave - 4;
  if (s < 5) {
    lds_wait_ge(LV + C_VISS, 4);
    const int g4 = lane >> 4, z = lane & 15;
    const rsrc_t ro = mkrs(wo_ + (size_t)(h * DH + 24 * tw) * D + s * 128, 23 * D * 2 + 256);
    const int ooff = 6 * g4 * (D * 2) + z * 16;
    u32x4 wr[6];
    wait_until(tstart + TG_WO);
    const bool paced = __builtin_amdgcn_s_memrealtime() < tstart + TL_WO;
#pragma unroll
    for (int k = 0; k < 6; ++k) { if (k >= WOWIN && paced) vmw<WOWIN - 1>(); wr[k] = ld_nt(ro, ooff + k * (D * 2)); SCHED(); }
    u32x4 mA, mB;
#pragma unroll
    for (int d = 0; d < 4; ++d) { mA[d] = z < 8 ? msk[d] : 0u; mB[d] = z < 8 ? 0u : msk[d]; }
    int xw = L_X + tw * 288 + r8 * 36 + c8 * 4, xm = L_X + tw * 288 + c8 * 36 + 24, xo = L_X + tw * 288 + c8 * 36 + 3 * r8;
    int wadr = L_T + (tw * 2 + (g4 >> 1)) * 128 + z * 8 + (g4 & 1) * 4;
    int radr = L_T + r8 * 256 + tw * 32 + c8 * 4;
    int sadr = WS_YP + (h * D + s * 128 + tw * 32 + c8 * 4) * 4;
    PIN(mA); PIN(mB); PIN(xw); PIN(xm); PIN(xo); PIN(wadr); PIN(radr); PIN(sadr);
    lds_wait_ge(LV + C_PUB, 1);
    const int gb = WS_OP + (h * 8 + r8) * OP_REC + tw * 128 + c8 * 16;
    u32x4 va = {c8 == 6 ? 0xff800000u : 0u, tag & 0xffffu, 0u, tag & 0xffffu};
    bool ok = r8 == 7;
    wait_until(tstart + TG_O);
#pragma nounroll
    for (u32 sp = 0; sp < SPIN_CAP; ++sp) {
      CBAR();
      if (!ok) va = ld_sc1(rs, gb);
      ok = ok4(va, tag);
      if (__all(ok)) break;
    }
    *(LAS f32x4*)(L + xw) = unpack4(va);
    CBAR();
    const f32x2 ml = *(const LAS f32x2*)(L + xm);
    const float o0 = uf(L[xo]), o1 = uf(L[xo + 1]), o2 = uf(L[xo + 2]);
    SCHED();
    const float f = __builtin_amdgcn_exp2f((ml[0] - max8(ml[0])) * LOG2E);
    float t0 = f * o0, t1 = f * o1, t2 = f * o2, t3 = f * ml[1];
    t0 += dpp<XOR1>(t0); t1 += dpp<XOR1>(t1); t2 += dpp<XOR1>(t2); t3 += dpp<XOR1>(t3);
    t3 += dpp<XOR2>(t3); t0 += dpp<XOR2>(t0); t1 += dpp<XOR2>(t1); t2 += dpp<XOR2>(t2);
    t3 += dpp<HMIR>(t3); t0 += dpp<HMIR>(t0); t1 += dpp<HMIR>(t1); t2 += dpp<HMIR>(t2);
    const float li = __builtin_amdgcn_rcpf(t3);
    t0 *= li; t1 *= li; t2 *= li;
    const u32 v0 = pk_bf16(t0, t0), v1 = pk_bf16(t1, t1), v2 = pk_bf16(t2, t2);
    const f32x4 zero = {0.f, 0.f, 0.f, 0.f};
    f32x4 aA = mfma(sel(v0, mA), wr[0], zero), aB = mfma(sel(v0, mB), wr[3], zero);
    aA = mfma(sel(v1, mA), wr[1], aA); aB = mfma(sel(v1, mB), wr[4], aB);
    aA = mfma(sel(v2, mA), wr[2], aA);
    aA = mfma(sel(v2, mB), wr[5], aA);
    *(LAS f32x4*)(L + wadr) = aA + aB;
    WG_BARRIER();
    if (tw < 4) {
      const f32x4 t = sum_r8(*(const LAS f32x4*)(L + radr) + *(const LAS f32x4*)(L + radr + 128));
      const u32x4 pk = pack4(t, tag);
      if (lane < 8) st_sc1(rs, sadr, pk);
    }
    return;
  }

  const int fi = (idx >> 3) * 2 + (s - 5);
  if (fi < 6) {
    const int ft = xcd * 6 + fi, mat = ft & 1, tile = ft >> 1;
    lds_wait_ge(LV + C_VISS, 4);
    u32 gw[5][8];
    {
      const rsrc_t rg = mkrs((mat ? wu_ : wg_) + (size_t)80 * tw * F + tile * 64, 79 * F * 2 + 128);
      const int goff = 10 * r8 * (F * 2) + c8 * 16;
      u32x4 t[10];
      wait_until(tstart + TG_F);
#pragma unroll
      for (int i = 0; i < 10; ++i) {
        if (i == F_EARLY) { SCHED(); lds_wait_ge(LV + C_PUB, 1); SCHED(); }
        t[i] = ld_nt(rg, goff + i * (F * 2));
        SCHED();
      }
      SCHED();
#pragma unroll
      for (int i = 0; i < 5; ++i)
#pragma unroll
        for (int d = 0; d < 4; ++d) {
          gw[i][2 * d] = __builtin_amdgcn_perm(t[2 * i + 1][d], t[2 * i][d], 0x05040100u);
          gw[i][2 * d + 1] = __builtin_amdgcn_perm(t[2 * i + 1][d], t[2 * i][d], 0x07060302u);
        }
#pragma unroll
      for (int i = 0; i < 5; ++i)
#pragma unroll
        for (int c = 0; c < 8; ++c) PIN(gw[i][c]);
    }
    int wadr = L_T + (tw * 2 + ((lane >> 3) & 1)) * 64 + c8 * 8 + (lane >> 5) * 4 + ((lane >> 4) & 1) * 2;
    int radr = L_T + (lane >> 4) * 256 + (lane & 15) * 4;
    auto gemv = [&](const u32 (&hn)[5], float (&ac)[8]) {
#pragma unroll
      for (int i = 0; i < 5; ++i)
#pragma unroll
        for (int c = 0; c < 8; ++c) ac[c] = dot2(gw[i][c], hn[i], ac[c]);
      float b4[4];
#pragma unroll
      for (int c = 0; c < 4; ++c) b4[c] = tr32(ac[c], ac[c + 4]);
      *(LAS f32x2*)(L + wadr) = f32x2{tr16(b4[0], b4[2]), tr16(b4[1], b4[3])};
    };
    auto total = [&]() -> float {
      const f32x4 g = (*(const LAS f32x4*)(L + radr) + *(const LAS f32x4*)(L + radr + 64)) + (*(const LAS f32x4*)(L + radr + 128) + *(const LAS f32x4*)(L + radr + 192));
      return tr16(tr32(g[0], g[2]), tr32(g[1], g[3]));
    };
    float ac[8];
    {
      const u32 ones[5] = {0x3f803f80u, 0x3f803f80u, 0x3f803f80u, 0x3f803f80u, 0x3f803f80u};
#pragma unroll
      for (int c = 0; c < 8; ++c) ac[c] = 0.f;
      gemv(ones, ac);
    }
    WG_BARRIER();
    float cw = 0.f;
    if (tw == 0) cw = total();
    PIN(cw);
    WG_BARRIER();
#pragma unroll
    for (int c = 0; c < 8; ++c) { ac[c] = 0.f; PIN(ac[c]); }
    float e5 = c8 < 5 ? 1.0f : 0.0f;
    int sadr = WS_GU + (lane >> 4) * GU_REP + mat * 6144 + tile * 256 + (lane & 15) * 16;
    int stw = L_ST + tw, str = L_ST + c8;
    PIN(e5); PIN(sadr); PIN(wadr); PIN(radr); PIN(stw); PIN(str);
    u32x2 hv;
    {
      const int o0 = WS_HS + xcd * HS_REP + (40 * tw + 5 * r8 + (c8 < 5 ? c8 : 0)) * 8;
#pragma nounroll
      for (u32 sp = 0; sp < SPIN_CAP; ++sp) {
        CBAR();
        hv = ld_sc1_64(rs, o0);
        if (__all(hv[1] == tag)) break;
      }
    }
    u32 hn[5];
    hn[0] = __builtin_amdgcn_ds_swizzle(hv[0], 0x18 | (0 << 5));
    hn[1] = __builtin_amdgcn_ds_swizzle(hv[0], 0x18 | (1 << 5));
    hn[2] = __builtin_amdgcn_ds_swizzle(hv[0], 0x18 | (2 << 5));
    hn[3] = __builtin_amdgcn_ds_swizzle(hv[0], 0x18 | (3 << 5));
    hn[4] = __builtin_amdgcn_ds_swizzle(hv[0], 0x18 | (4 << 5));
    const float pv = lo_f(__builtin_amdgcn_readfirstlane(hv[0]));
    const float d0 = (lo_f(hv[0]) - pv) * e5, d1 = (hi_f(hv[0]) - pv) * e5;
    float s1 = sum8(d0 + d1), s2 = sum8(d0 * d0 + d1 * d1);
    s1 += dpp<ROR8>(s1); s2 += dpp<ROR8>(s2);
    float ss = tr32(s1, s2);
    ss = tr16(ss, ss);
    gemv(hn, ac);
    {
      auto sw = __builtin_amdgcn_permlane32_swap(fu(ss), fu(ss), false, false);
      const float t1 = uf(sw[0]), t2 = uf(sw[1]);
      if (lane == 0) { L[stw] = fu(pv + t1 * (1.0f / 80.0f)); L[stw + 8] = fu(t2 - t1 * t1 * (1.0f / 80.0f)); }
    }
    WG_BARRIER();
    if (tw == 0) {
      const float mw = uf(L[str]), qw = uf(L[str + 8]);
      const float G = total();
      const float mean = sum8(mw) * 0.125f;
      const float var = rbf(sum8(qw + 80.0f * (mw - mean) * (mw - mean)) * (1.0f / D));
      const float rinv = __builtin_amdgcn_rcpf(rbf(__builtin_amdgcn_sqrtf(rbf(var + 1e-5f))));
      const float g = (G - rbf(mean) * cw) * rinv;
      auto sa = __builtin_amdgcn_permlane16_swap(fu(g), fu(g), false, false);
      const u32 pr = pk_bf16(uf(sa[0]), uf(sa[1]));
      auto sb = __builtin_amdgcn_permlane32_swap(pr, pr, false, false);
      st_sc1(rs, sadr, u32x4{sb[0], tag, sb[1], tag});
    }
  }
}

extern "C" void kernel_launch(void* const* d_in, const int* in_sizes, int n_in,
                              void* d_out, int out_size, void* d_ws, size_t ws_size,
                              hipStream_t stream) {
  hipLaunchKernelGGL(fused_block, dim3(256), dim3(NTHR), 0, stream,
                     (const u16*)d_in[0], (const u16*)d_in[1], (const u16*)d_in[2], (const u16*)d_in[3], (const u16*)d_in[4],
                     (const u16*)d_in[6], (const u16*)d_in[7], (const u16*)d_in[8], (u16*)d_out, (unsigned char*)d_ws);
}
```

```cpp
#include <hip/hip_runtime.h>
#include <hip/hip_bf16.h>
#include <stdint.h>

typedef unsigned short u16;
typedef unsigned u32;
typedef unsigned long long u64;
typedef u32 u32x4 __attribute__((ext_vector_type(4)));
typedef u32 u32x2 __attribute__((ext_vector_type(2)));
typedef float f32x4 __attribute__((ext_vector_type(4)));
typedef float f32x2 __attribute__((ext_vector_type(2)));
typedef __bf16 bf16x8 __attribute__((ext_vector_type(8)));
typedef __bf16 bf16x2 __attribute__((ext_vector_type(2)));
#define GAS __attribute__((address_space(1)))
#define LAS __attribute__((address_space(3)))

constexpr int D = 640, H = 32, DH = 192, SK = 8192, W = 2048, F = 1536, HD = H * DH;
constexpr int NTHR = 768;
constexpr float LOG2E = 1.4426950408889634f;

constexpr int WIN = 8;
constexpr int WVA = 9, WVB = 7;
constexpr int KGATE = 24;
constexpr int WOWIN = 3;
constexpr int F_EARLY = 4;
constexpr int TG_QP = 395, TG_Q = 495, TG_WO = 795, TG_F = 805, TG_D = 915, TG_P = 965, TG_O = 1035, TG_R = 1175;
constexpr int LI1 = 22, LT1 = 670, LWIN = 14, TL_WO = 840;
constexpr int PF_BYTES = 15360, PF_REP = 3, PF_T0 = 100, PF_PER = 350;

constexpr int WS_EPOCH = 0;
constexpr int HS_REP = 2560;
constexpr int WS_HS = 4096;
constexpr int GU_REP = 12288;
constexpr int WS_GU = 32768;
constexpr int WS_YP = WS_GU + 4 * GU_REP;
constexpr int WS_QP = WS_YP + H * D * 4;
constexpr int OP_REC = 1024;
constexpr int WS_OP = WS_QP + H * 8 * DH * 4;
constexpr int WS_END = WS_OP + H * 8 * OP_REC;
constexpr u32 SPIN_CAP = 1u << 21;

constexpr int C_Q = 0, C_KISS = 1, C_VISS = 2, C_O = 3, C_P = 4, C_PUB = 5, C_TAG = 6, C_PF = 8;
constexpr int L_Q = 16;
constexpr int L_P = 112;
constexpr int L_ML = L_P + 384;
constexpr int L_O = L_ML + 8;
constexpr int L_T = L_O + 8 * 192;
constexpr int L_ST = L_T + 2048;
constexpr int L_X = L_ST + 16;
constexpr int L_END = L_X + 8 * 288;

__device__ __forceinline__ u32 fu(float f) { return __float_as_uint(f); }
__device__ __forceinline__ float uf(u32 u) { return __uint_as_float(u); }
__device__ __forceinline__ float lo_f(u32 u) { return uf(u << 16); }
__device__ __forceinline__ float hi_f(u32 u) { return uf(u & 0xffff0000u); }
__device__ __forceinline__ u32 pk_bf16(float lo, float hi) {
  bf16x2 v = {(__bf16)lo, (__bf16)hi};
  return __builtin_bit_cast(u32, v);
}
__device__ __forceinline__ float rbf(float f) { return (float)(__bf16)f; }
__device__ __forceinline__ float dot2(u32 a, u32 b, float acc) {
  return __builtin_amdgcn_fdot2_f32_bf16(__builtin_bit_cast(bf16x2, a), __builtin_bit_cast(bf16x2, b), acc, false);
}
__device__ __forceinline__ f32x4 mfma(u32x4 a, u32x4 b, f32x4 c) {
  return __builtin_amdgcn_mfma_f32_16x16x32_bf16(__builtin_bit_cast(bf16x8, a), __builtin_bit_cast(bf16x8, b), c, 0, 0, 0);
}
__device__ __forceinline__ u32x4 sel(u32 v, u32x4 m) { return u32x4{v & m[0], v & m[1], v & m[2], v & m[3]}; }
template <int CTRL> __device__ __forceinline__ float dpp(float x) {
  return __builtin_bit_cast(float, __builtin_amdgcn_mov_dpp(__builtin_bit_cast(int, x), CTRL, 0xf, 0xf, true));
}
template <int CTRL> __device__ __forceinline__ u32 dppu(u32 x) { return (u32)__builtin_amdgcn_mov_dpp((int)x, CTRL, 0xf, 0xf, true); }
constexpr int XOR1 = 0xB1, XOR2 = 0x4E, HMIR = 0x141, ROR8 = 0x128;
__device__ __forceinline__ float sum8(float x) { x += dpp<XOR1>(x); x += dpp<XOR2>(x); x += dpp<HMIR>(x); return x; }
__device__ __forceinline__ float max8(float x) {
  float r;
  asm volatile("s_nop 1\n\tv_max_f32_dpp %0, %1, %1 quad_perm:[1,0,3,2] row_mask:0xf bank_mask:0xf\n\t"
               "s_nop 1\n\tv_max_f32_dpp %0, %0, %0 quad_perm:[2,3,0,1] row_mask:0xf bank_mask:0xf\n\t"
               "s_nop 1\n\tv_max_f32_dpp %0, %0, %0 row_half_mirror row_mask:0xf bank_mask:0xf" : "=&v"(r) : "v"(x));
  return r;
}
__device__ __forceinline__ float max16(float x) {
  float r;
  asm volatile("s_nop 1\n\tv_max_f32_dpp %0, %1, %1 quad_perm:[1,0,3,2] row_mask:0xf bank_mask:0xf\n\t"
               "s_nop 1\n\tv_max_f32_dpp %0, %0, %0 quad_perm:[2,3,0,1] row_mask:0xf bank_mask:0xf\n\t"
               "s_nop 1\n\tv_max_f32_dpp %0, %0, %0 row_half_mirror row_mask:0xf bank_mask:0xf\n\t"
               "s_nop 1\n\tv_max_f32_dpp %0, %0, %0 row_ror:8 row_mask:0xf bank_mask:0xf\n\ts_nop 1" : "=&v"(r) : "v"(x));
  return r;
}
__device__ __forceinline__ float tr32(float a, float b) {
  auto t = __builtin_amdgcn_permlane32_swap(fu(a), fu(b), false, false);
  return uf(t[0]) + uf(t[1]);
}
__device__ __forceinline__ float tr16(float a, float b) {
  auto t = __builtin_amdgcn_permlane16_swap(fu(a), fu(b), false, false);
  return uf(t[0]) + uf(t[1]);
}
__device__ __forceinline__ float xrow_sum(float x) { x = tr16(x, x); return tr32(x, x); }
__device__ __forceinline__ float xrow_max(float x) {
  auto s = __builtin_amdgcn_permlane16_swap(fu(x), fu(x), false, false);
  x = fmaxf(uf(s[0]), uf(s[1]));
  auto t = __builtin_amdgcn_permlane32_swap(fu(x), fu(x), false, false);
  return fmaxf(uf(t[0]), uf(t[1]));
}
__device__ __forceinline__ float sum64(float x) { x = sum8(x); x += dpp<ROR8>(x); return xrow_sum(x); }
__device__ __forceinline__ f32x4 sum_r8(f32x4 t) {
  float w = tr16(tr32(t[0], t[1]), tr32(t[2], t[3]));
  w += dpp<ROR8>(w);
  auto sa = __builtin_amdgcn_permlane16_swap(fu(w), fu(w), false, false);
  auto se = __builtin_amdgcn_permlane32_swap(sa[0], sa[0], false, false);
  auto so = __builtin_amdgcn_permlane32_swap(sa[1], sa[1], false, false);
  return f32x4{uf(se[0]), uf(se[1]), uf(so[0]), uf(so[1])};
}

typedef __amdgpu_buffer_rsrc_t rsrc_t;
__device__ __forceinline__ rsrc_t mkrs(const void* p, int bytes) { return __builtin_amdgcn_make_buffer_rsrc((void*)p, 0, bytes, 0x00020000); }
__device__ __forceinline__ u32x4 ld_nt(rsrc_t rs, int off) { return __builtin_amdgcn_raw_buffer_load_b128(rs, off, 0, 2); }
__device__ __forceinline__ u32x4 ld_sc1(rsrc_t rs, int off) { return __builtin_amdgcn_raw_buffer_load_b128(rs, off, 0, 16); }
__device__ __forceinline__ u32x2 ld_sc1_64(rsrc_t rs, int off) { return __builtin_amdgcn_raw_buffer_load_b64(rs, off, 0, 16); }
__device__ __forceinline__ void st_sc1(rsrc_t rs, int off, u32x4 v) { __builtin_amdgcn_raw_buffer_store_b128(v, rs, off, 0, 16); }
template <int N0, int N1, int N2> __device__ __forceinline__ void vmw3(int mode) {
  asm volatile("s_cmp_eq_u32 %0, 2\n\ts_cbranch_scc1 1f\n\ts_waitcnt vmcnt(%2)\n\ts_cmp_eq_u32 %0, 1\n\ts_cbranch_scc1 1f\n\t"
               "s_waitcnt vmcnt(%1)\n1:\n\ts_waitcnt vmcnt(%3)" :: "s"(mode), "i"(N0), "i"(N1), "i"(N2) : "memory", "scc");
}
template <int N> __device__ __forceinline__ void vmw() { asm volatile("s_waitcnt vmcnt(%0)" :: "i"(N) : "memory"); }

__device__ __forceinline__ u32x4 pack4(f32x4 v, u32 tag) {
  const u32 a = fu(v[0]) + 0x80u, b = fu(v[1]) + 0x80u, c = fu(v[2]) + 0x80u, d = fu(v[3]) + 0x80u;
  return u32x4{__builtin_amdgcn_perm(a, b, 0x07060503u), __builtin_amdgcn_perm(b, tag, 0x06050100u),
               __builtin_amdgcn_perm(c, d, 0x07060503u), __builtin_amdgcn_perm(d, tag, 0x06050100u)};
}
__device__ __forceinline__ f32x4 unpack4(u32x4 w) {
  return f32x4{uf(w[0] & 0xffffff00u), uf(__builtin_amdgcn_perm(w[0], w[1], 0x0403020cu)),
               uf(w[2] & 0xffffff00u), uf(__builtin_amdgcn_perm(w[2], w[3], 0x0403020cu))};
}
__device__ __forceinline__ bool ok4(u32x4 w, u32 tag) { return (((w[1] ^ tag) | (w[3] ^ tag)) & 0xffffu) == 0u; }

#define CBAR() asm volatile("" ::: "memory")
#define PIN(x) asm volatile("" : "+v"(x))
#define LGKM0() asm volatile("s_waitcnt lgkmcnt(0)" ::: "memory")
#define WG_BARRIER() asm volatile("s_waitcnt lgkmcnt(0)\n\ts_barrier" ::: "memory")
#define SCHED() __builtin_amdgcn_sched_barrier(0)

__device__ __forceinline__ void lds_wait_ge(volatile LAS u32* w, u32 n) {
#pragma nounroll
  for (u32 sp = 0; sp < SPIN_CAP; ++sp) {
    if (*w >= n) break;
    __builtin_amdgcn_s_sleep(1);
  }
  CBAR();
}
__device__ __forceinline__ void wait_until(u64 t) {
#pragma nounroll
  for (u32 sp = 0; sp < 4096; ++sp) {
    if (__builtin_amdgcn_s_memrealtime() >= t) break;
    __builtin_amdgcn_s_sleep(1);
  }
  CBAR();
}
__device__ __forceinline__ void lds_inc(LAS u32* w, int lane) {
  CBAR();
  if (lane == 0) __hip_atomic_fetch_add(w, 1u, __ATOMIC_RELAXED, __HIP_MEMORY_SCOPE_WORKGROUP);
  CBAR();
}

__global__ void __launch_bounds__(NTHR, 3)
fused_block(const u16* __restrict__ x_, const u16* __restrict__ kc_, const u16* __restrict__ vc_, const u16* __restrict__ wq_,
            const u16* __restrict__ wo_, const u16* __restrict__ wg_, const u16* __restrict__ wu_, const u16* __restrict__ wd_,
            u16* __restrict__ out_, unsigned char* ws_) {
  u64 pc0; asm volatile("s_getpc_b64 %0" : "=s"(pc0));
  __shared__ __attribute__((aligned(16))) u32 smem[L_END];
  LAS u32* L = (LAS u32*)smem;
  volatile LAS u32* LV = (volatile LAS u32*)smem;
  const int tid = threadIdx.x, lane = tid & 63, wave = __builtin_amdgcn_readfirstlane(tid >> 6);
  const int b = blockIdx.x;
  const int xcd = b & 7, idx = b >> 3;
  const int h = xcd * 4 + (idx >> 3), s = idx & 7;
  rsrc_t rs;
  const int r8 = lane >> 3, c8 = lane & 7;
  u64 tstart;
  u32x4 msk;
#pragma unroll
  for (int d = 0; d < 4; ++d) msk[d] = (d == (c8 >> 1)) ? ((c8 & 1) ? 0xffff0000u : 0x0000ffffu) : 0u;
  u32 tag;
#define START() do { unsigned char* w_ = ws_; asm volatile("" : "+s"(w_) : "s"(x_), "s"(kc_), "s"(vc_), "s"(wq_), "s"(wo_), "s"(wg_), "s"(wu_), "s"(wd_), "s"(out_)); \
    rs = mkrs(w_, WS_END); tstart = __builtin_amdgcn_s_memrealtime(); SCHED(); } while (0)
#define FIRST_BARRIER() do { WG_BARRIER(); tag = __builtin_amdgcn_readfirstlane(LV[C_TAG]); } while (0)
  const int g0 = 36 * s + (s > 3 ? s - 3 : 0), ng = s >= 3 ? 37 : 36;
  const size_t kvoff = ((size_t)h * SK + (SK - W) + g0 * 8) * DH;

  if (wave < 8 && s == 7) {
    START();
    FIRST_BARRIER();
    if (wave == 2 && idx >= 16) {
      const rsrc_t rc = mkrs((const void*)(pc0 & ~127ull), PF_BYTES);
      const int off = ((3 - (idx >> 3)) * 64 + lane) * 128;
#pragma nounroll
      for (int r = 0; r < PF_REP; ++r) {
        wait_until(tstart + PF_T0 + r * PF_PER);
        u32 t1 = __builtin_amdgcn_raw_buffer_load_b32(rc, off, 0, 2);
        asm volatile("" :: "v"(t1));
      }
    }
  } else if (wave < 4) {
    __builtin_amdgcn_s_setprio(2);
    START();
    const rsrc_t rk = mkrs(kc_ + kvoff, ng * 3072);
    const int loff = wave * 3072 + r8 * 384 + c8 * 16;
    u32x4 kr[30];
#pragma unroll
    for (int i = 0; i < 30; ++i) {
      if (i == WIN) FIRST_BARRIER();
      if (i >= WIN) vmw<WIN - 1>();
      if (i == KGATE) lds_inc(L + C_KISS, lane);
      kr[i] = ld_nt(rk, loff + (i / 3) * 12288 + (i % 3) * 128);
      SCHED();
    }
    float dd[10];
    const bool sbok = c8 == 0 || (c8 == 1 && 36 + wave < ng);
    int padr = L_P + wave * 96 + r8 * 12 + c8, padr2 = c8 < 2 ? padr + 8 : L_T + lane;
    PIN(padr); PIN(padr2);
#pragma unroll
    for (int g = 0; g < 10; ++g) { dd[g] = 0.f; PIN(dd[g]); }
    lds_wait_ge(LV + C_Q, 3);
    u32x4 qp[3];
#pragma unroll
    for (int t = 0; t < 3; ++t) qp[t] = *(const LAS u32x4*)(L + L_Q + t * 32 + c8 * 4);
#pragma unroll
    for (int e = 0; e < 12; ++e) {
#pragma unroll
      for (int g = 0; g < 10; ++g) dd[g] = dot2(kr[g * 3 + e / 4][e % 4], qp[e / 4][e % 4], dd[g]);
      SCHED();
    }
    const bool b0 = c8 & 1, b1 = c8 & 2, b2 = c8 & 4;
    float e4[4], e2[2];
#pragma unroll
    for (int i = 0; i < 4; ++i) { const float keep = b2 ? dd[i + 4] : dd[i], give = b2 ? dd[i] : dd[i + 4]; e4[i] = keep + dpp<HMIR>(give); }
#pragma unroll
    for (int i = 0; i < 2; ++i) { const float keep = b1 ? e4[i + 2] : e4[i], give = b1 ? e4[i] : e4[i + 2]; e2[i] = keep + dpp<XOR2>(give); }
    float sa = (b0 ? e2[1] : e2[0]) + dpp<XOR1>(b0 ? e2[0] : e2[1]);
    float sb = (b0 ? dd[9] : dd[8]) + dpp<XOR1>(b0 ? dd[8] : dd[9]);
    sb += dpp<XOR2>(sb);
    sb += dpp<0x1B>(dpp<HMIR>(sb));
    sa = rbf(rbf(sa) * 0.07216878364870323f);
    sb = rbf(rbf(sb) * 0.07216878364870323f);
    sb = sbok ? sb : -INFINITY;
    const float m = xrow_max(max16(fmaxf(sa, sb)));
    const float pa = __builtin_amdgcn_exp2f((sa - m) * LOG2E), pb = __builtin_amdgcn_exp2f((sb - m) * LOG2E);
    L[padr] = pk_bf16(pa, pa);
    L[padr2] = pk_bf16(pb, pb);
    CBAR();
    if (lane == 0) LV[C_PF + wave] = 1u;
    const float l = sum64(pa + pb);
    if (lane == 0) { L[L_ML + wave] = fu(m); L[L_ML + 4 + wave] = fu(l); }
    lds_inc(L + C_P, lane);
  } else if (wave < 8) {
    __builtin_amdgcn_s_setprio(1);
    START();
    const int w = wave - 4;
    const rsrc_t rv = mkrs(vc_ + kvoff, ng * 3072);
    const int loff = w * 3072 + r8 * 384 + c8 * 16;
    FIRST_BARRIER();
    lds_wait_ge(LV + C_KISS, 4);
    u32x4 vr[30];
    int mode = __builtin_amdgcn_readfirstlane(s < 5 ? 1 : 0);
#pragma unroll
    for (int i = 0; i < 30; ++i) {
      const int j = (i + 27) % 30;
      if (i == LI1) mode = __builtin_amdgcn_readfirstlane(__builtin_amdgcn_s_memrealtime() > tstart + LT1 ? 2 : mode);
      if (i >= WVB) vmw3<WVB - 1, WVA - 1, LWIN - 1>(mode);
      if (i == 29) lds_inc(L + C_VISS, lane);
      vr[j] = ld_nt(rv, loff + (j / 3) * 12288 + (j % 3) * 128);
      SCHED();
    }
    int wadr = ((lane >> 5) == ((lane >> 3) & 1) ? L_O + (w * 2 + (lane >> 5)) * 192 : L_T + 64 + w * 192) + c8 * 8 + ((lane >> 4) & 1) * 4;
    int radr = L_O + 24 * r8 + 4 * (c8 < 6 ? c8 : 0);
    int sadr = WS_OP + (h * 8 + s) * OP_REC + lane * 16;
    u32 tadr = (u32)(uintptr_t)(L + C_O), one = 1u;
    PIN(wadr); PIN(radr); PIN(sadr); PIN(tadr); PIN(one);
    lds_wait_ge(LV + C_PF + w, 1);
    const LAS u32* pr = L + L_P + w * 96 + r8 * 12;
    const u32x4 p0 = *(const LAS u32x4*)(pr), p1 = *(const LAS u32x4*)(pr + 4);
    const u32x2 p2 = *(const LAS u32x2*)(pr + 8);
    float M, Ls, fw;
    f32x4 acc[2][3];
#pragma unroll
    for (int gg = 0; gg < 10; ++gg) {
      const int g = (gg + 9) % 10;
      if (gg == 5) {
        SCHED();
        lds_wait_ge(LV + C_P, 4);
        const f32x4 mm = *(const LAS f32x4*)(L + L_ML), ll = *(const LAS f32x4*)(L + L_ML + 4);
        M = fmaxf(fmaxf(mm[0], mm[1]), fmaxf(mm[2], mm[3]));
        f32x4 ff;
#pragma unroll
        for (int k = 0; k < 4; ++k) ff[k] = __builtin_amdgcn_exp2f((mm[k] - M) * LOG2E);
        Ls = (ff[0] * ll[0] + ff[1] * ll[1]) + (ff[2] * ll[2] + ff[3] * ll[3]);
        fw = w == 0 ? ff[0] : (w == 1 ? ff[1] : (w == 2 ? ff[2] : ff[3]));
        PIN(M); PIN(Ls); PIN(fw);
      }
      const u32x4 A = sel(g < 4 ? p0[g & 3] : (g < 8 ? p1[g & 3] : p2[g & 1]), msk);
#pragma unroll
      for (int t = 0; t < 3; ++t) {
        if (gg == 9) acc[0][t] = mfma(A, vr[g * 3 + t], acc[0][t] + acc[1][t]);
        else acc[gg & 1][t] = mfma(A, vr[g * 3 + t], gg < 2 ? f32x4{0.f, 0.f, 0.f, 0.f} : acc[gg & 1][t]);
      }
    }
#pragma unroll
    for (int t = 0; t < 3; ++t) *(LAS f32x4*)(L + wadr + t * 64) = acc[0][t] * fw;
    u32 old;
    asm volatile("s_mov_b64 exec, 1\n\tds_add_rtn_u32 %0, %1, %2\n\ts_mov_b64 exec, -1\n\ts_waitcnt lgkmcnt(0)" : "=&v"(old) : "v"(tadr), "v"(one) : "memory");
    if (__builtin_amdgcn_readfirstlane(old) == 3) {
      const u64 tnow = __builtin_amdgcn_s_memrealtime();
      f32x4 pt[8];
#pragma unroll
      for (int k = 0; k < 8; ++k) pt[k] = *(const LAS f32x4*)(L + radr + k * 192);
      SCHED();
      f32x4 o = ((pt[0] + pt[1]) + (pt[2] + pt[3])) + ((pt[4] + pt[5]) + (pt[6] + pt[7]));
      if (c8 == 6) { o[0] = M; o[1] = Ls; }
      u32x4 pk = pack4(o, tag);
      PIN(pk);
      if (tnow < tstart + TG_P) wait_until(tstart + TG_P);
      st_sc1(rs, sadr, pk);
      if (lane == 0) LV[C_PUB] = 1u;
    }
    return;
  } else {
    const int a = wave - 8;
    __builtin_amdgcn_s_setprio(3);
    START();
    if (a < 3) {
      const GAS char* xb = (const GAS char*)x_;
      const u32x4 xa = *(const GAS u32x4*)(xb + lane * 16);
      const u32 xc = *(const GAS u32*)(xb + 1024 + lane * 4);
      const GAS u32* xr = (const GAS u32*)(xb + 160 * s + 20 * r8);
      u32 xo[5];
#pragma unroll
      for (int i = 0; i < 5; ++i) xo[i] = xr[i];
      const rsrc_t rq = mkrs(wq_ + (size_t)80 * s * HD + h * DH + a * 64, 79 * HD * 2 + 128);
      const int qoff = 10 * r8 * (HD * 2) + c8 * 16;
      u32x4 wr[10];
#pragma unroll
      for (int g = 0; g < 10; ++g) wr[g] = ld_nt(rq, qoff + g * (HD * 2));
      SCHED();
      FIRST_BARRIER();
      SCHED();
      const float xv[10] = {lo_f(xa[0]), hi_f(xa[0]), lo_f(xa[1]), hi_f(xa[1]), lo_f(xa[2]), hi_f(xa[2]), lo_f(xa[3]), hi_f(xa[3]), lo_f(xc), hi_f(xc)};
      float sm = 0.f;
#pragma unroll
      for (int i = 0; i < 10; ++i) sm += xv[i];
      const float mean = sum64(sm) * (1.0f / D);
      float sq = 0.f;
#pragma unroll
      for (int i = 0; i < 10; ++i) { const float dd = xv[i] - mean; sq += dd * dd; }
      const float var = rbf(sum64(sq) * (1.0f / D));
      const float mb = rbf(mean);
      const float rinv = 1.0f / rbf(__builtin_sqrtf(rbf(var + 1e-5f)));
      f32x4 acc = {0.f, 0.f, 0.f, 0.f};
#pragma unroll
      for (int g = 0; g < 10; ++g) {
        const float xg = (g & 1) ? hi_f(xo[g >> 1]) : lo_f(xo[g >> 1]);
        const float xn = rbf(xg - mb) * rinv;
        acc = mfma(sel(pk_bf16(xn, xn), msk), wr[g], acc);
      }
#pragma unroll
      for (int i = 0; i < 4; ++i) {
        const float up = dpp<ROR8>(acc[i]);
        auto t = __builtin_amdgcn_permlane32_swap(fu(up), fu(up), false, false);
        acc[i] += uf(t[1]);
      }
      u32x4 pk = pack4(acc, tag);
      PIN(pk);
      wait_until(tstart + TG_QP);
      if (lane < 32 && (lane & 8) == 0) st_sc1(rs, WS_QP + (h * 8 + s) * (DH * 4) + a * 256 + (c8 + 8 * (lane >> 4)) * 16, pk);
      if (s < 7) {
        const int gb = WS_QP + (h * 8 + (lane >> 4)) * (DH * 4) + a * 256 + (lane & 15) * 16;
        u32x4 v[2];
        bool ok[2] = {false, false};
        wait_until(tstart + TG_Q);
#pragma nounroll
        for (u32 sp = 0; sp < SPIN_CAP; ++sp) {
          CBAR();
#pragma unroll
          for (int i = 0; i < 2; ++i) if (!ok[i]) v[i] = ld_sc1(rs, gb + i * (4 * DH * 4));
#pragma unroll
          for (int i = 0; i < 2; ++i) ok[i] = ok4(v[i], tag);
          if (__all(ok[0] & ok[1])) break;
          __builtin_amdgcn_s_sleep(2);
        }
        f32x4 q = unpack4(v[0]) + unpack4(v[1]);
#pragma unroll
        for (int i = 0; i < 4; ++i) q[i] = xrow_sum(q[i]);
        if (lane < 16) *(LAS u32x2*)(L + L_Q + a * 32 + 4 * c8 + 2 * (lane >> 3)) = u32x2{pk_bf16(q[0], q[1]), pk_bf16(q[2], q[3])};
        lds_inc(L + C_Q, lane);
      }
    } else {
      u32 epw;
      asm volatile("s_load_dword %0, %1, 0x0\n\ts_waitcnt lgkmcnt(0)" : "=&s"(epw) : "s"(ws_) : "memory");
      if (lane < 16) L[lane] = lane == C_TAG ? epw + 1u : 0u;
      FIRST_BARRIER();
    }
  }
  __builtin_amdgcn_s_setprio(0);

  if (s == 7) {
    const int j4 = idx >> 3;
    const int rI = j4 < 2 ? 2 * xcd + j4 : 14 + 2 * xcd + j4;
    if (rI < 20) {
      const int dt = rI;
      const int r16 = lane >> 2, c4 = lane & 3;
      u32 dw[4][8];
      {
        const rsrc_t rd = mkrs(wd_ + (size_t)128 * wave * D + dt * 32, 127 * D * 2 + 64);
        const int doff = 8 * r16 * (D * 2) + c4 * 16;
        wait_until(tstart + TG_D);
        u32x4 t[8];
#pragma unroll
        for (int i = 0; i < 8; ++i) { t[i] = ld_nt(rd, doff + i * (D * 2)); SCHED(); }
#pragma unroll
        for (int k = 0; k < 4; ++k)
#pragma unroll
          for (int d = 0; d < 4; ++d) {
            dw[k][2 * d] = __builtin_amdgcn_perm(t[2 * k + 1][d], t[2 * k][d], 0x05040100u);
            dw[k][2 * d + 1] = __builtin_amdgcn_perm(t[2 * k + 1][d], t[2 * k][d], 0x07060302u);
          }
#pragma unroll
        for (int k = 0; k < 4; ++k)
#pragma unroll
          for (int c = 0; c < 8; ++c) PIN(dw[k][c]);
      }
      float ac[8];
#pragma unroll
      for (int c = 0; c < 8; ++c) { ac[c] = 0.f; PIN(ac[c]); }
      int wadr = L_T + (wave * 4 + ((lane >> 2) & 3)) * 32 + c4 * 8 + (lane >> 5) * 4 + ((lane >> 4) & 1) * 2;
      int radr = L_T + r8 * 192 + c8 * 4;
      int oadr = dt * 64 + c8 * 8;
      PIN(wadr); PIN(radr); PIN(oadr);
      const rsrc_t ro = mkrs(out_, D * 2);
      u32x2 gv, uv;
      u32x4 hv = {0u, tag, 0u, tag};
      {
        const int o0 = WS_GU + ((rI >> 1) & 3) * GU_REP + (64 * wave + lane) * 8;
        const int oh = WS_HS + xcd * HS_REP + (dt * 16 + c8 * 2) * 8;
#pragma nounroll
        for (u32 sp = 0; sp < SPIN_CAP; ++sp) {
          CBAR();
          gv = ld_sc1_64(rs, o0); uv = ld_sc1_64(rs, o0 + 6144);
          if (wave == 0) hv = ld_sc1(rs, oh);
          if (__all(((gv[1] ^ tag) | (uv[1] ^ tag) | (hv[1] ^ tag) | (hv[3] ^ tag)) == 0u)) break;
        }
      }
      auto glu = [](float g, float u) -> float { return rbf(__builtin_amdgcn_rcpf(1.0f + __builtin_amdgcn_exp2f(-g * LOG2E))) * u; };
      const u32 ha = pk_bf16(glu(lo_f(gv[0]), lo_f(uv[0])), glu(hi_f(gv[0]), hi_f(uv[0])));
      const u32 hh[4] = {dppu<0x00>(ha), dppu<0x55>(ha), dppu<0xAA>(ha), dppu<0xFF>(ha)};
#pragma unroll
      for (int k = 0; k < 4; ++k)
#pragma unroll
        for (int c = 0; c < 8; ++c) ac[c] = dot2(dw[k][c], hh[k], ac[c]);
      float b4[4];
#pragma unroll
      for (int c = 0; c < 4; ++c) b4[c] = tr32(ac[c], ac[c + 4]);
      *(LAS f32x2*)(L + wadr) = f32x2{tr16(b4[0], b4[2]), tr16(b4[1], b4[3])};
      WG_BARRIER();
      if (wave == 0) {
        f32x4 p[6];
#pragma unroll
        for (int k = 0; k < 6; ++k) p[k] = *(const LAS f32x4*)(L + radr + k * 32);
        const f32x4 t = sum_r8(((p[0] + p[1]) + (p[2] + p[3])) + (p[4] + p[5]));
        const u32 d0 = pk_bf16(t[0], t[1]), d1 = pk_bf16(t[2], t[3]);
        const u32x2 ov = {pk_bf16(lo_f(hv[0]) + lo_f(d0), hi_f(hv[0]) + hi_f(d0)), pk_bf16(lo_f(hv[2]) + lo_f(d1), hi_f(hv[2]) + hi_f(d1))};
        if (lane < 8) __builtin_amdgcn_raw_buffer_store_b64(ov, ro, oadr, 0, 16);
        if (rI == 0 && lane == 0) __builtin_amdgcn_raw_buffer_store_b32(tag, rs, WS_EPOCH, 0, 16);
      }
    } else if (rI < 30 && wave < 2) {
      const int ru = (rI - 20) * 2 + wave;
      const u32x2 xp = *(const GAS u32x2*)((const GAS char*)x_ + (ru * 32 + c8 * 4) * 2);
      f32x4 xf = {lo_f(xp[0]), hi_f(xp[0]), lo_f(xp[1]), hi_f(xp[1])};
      int sadr = WS_HS + r8 * HS_REP + (ru * 16 + c8 * 2) * 8;
      PIN(xf); PIN(sadr);
      const int gb = WS_YP + (r8 * D + ru * 32 + c8 * 4) * 4;
      u32x4 v[4];
      bool ok[4] = {false, false, false, false};
      wait_until(tstart + TG_R);
#pragma nounroll
      for (u32 sp = 0; sp < SPIN_CAP; ++sp) {
        CBAR();
#pragma unroll
        for (int i = 0; i < 4; ++i) if (!ok[i]) v[i] = ld_sc1(rs, gb + i * (8 * D * 4));
#pragma unroll
        for (int i = 0; i < 4; ++i) ok[i] = ok4(v[i], tag);
        if (__all(ok[0] & ok[1] & ok[2] & ok[3])) break;
      }
      const f32x4 y = sum_r8((unpack4(v[0]) + unpack4(v[1])) + (unpack4(v[2]) + unpack4(v[3])));
      const u32 a0 = pk_bf16(y[0], y[1]), a1 = pk_bf16(y[2], y[3]);
      st_sc1(rs, sadr, u32x4{pk_bf16(xf[0] + lo_f(a0), xf[1] + hi_f(a0)), tag, pk_bf16(xf[2] + lo_f(a1), xf[3] + hi_f(a1)), tag});
    }
    return;
  }

  const int tw = wave < 4 ? wave : wave - 4;
  if (s < 5) {
    lds_wait_ge(LV + C_VISS, 4);
    const int g4 = lane >> 4, z = lane & 15;
    const rsrc_t ro = mkrs(wo_ + (size_t)(h * DH + 24 * tw) * D + s * 128, 23 * D * 2 + 256);
    const int ooff = 6 * g4 * (D * 2) + z * 16;
    u32x4 wr[6];
    wait_until(tstart + TG_WO);
    const bool paced = __builtin_amdgcn_s_memrealtime() < tstart + TL_WO;
#pragma unroll
    for (int k = 0; k < 6; ++k) { if (k >= WOWIN && paced) vmw<WOWIN - 1>(); wr[k] = ld_nt(ro, ooff + k * (D * 2)); SCHED(); }
    u32x4 mA, mB;
#pragma unroll
    for (int d = 0; d < 4; ++d) { mA[d] = z < 8 ? msk[d] : 0u; mB[d] = z < 8 ? 0u : msk[d]; }
    int xw = L_X + tw * 288 + r8 * 36 + c8 * 4, xm = L_X + tw * 288 + c8 * 36 + 24, xo = L_X + tw * 288 + c8 * 36 + 3 * r8;
    int wadr = L_T + (tw * 2 + (g4 >> 1)) * 128 + z * 8 + (g4 & 1) * 4;
    int radr = L_T + r8 * 256 + tw * 32 + c8 * 4;
    int sadr = WS_YP + (h * D + s * 128 + tw * 32 + c8 * 4) * 4;
    PIN(mA); PIN(mB); PIN(xw); PIN(xm); PIN(xo); PIN(wadr); PIN(radr); PIN(sadr);
    lds_wait_ge(LV + C_PUB, 1);
    const int gb = WS_OP + (h * 8 + r8) * OP_REC + tw * 128 + c8 * 16;
    u32x4 va = {c8 == 6 ? 0xff800000u : 0u, tag & 0xffffu, 0u, tag & 0xffffu};
    bool ok = r8 == 7;
    wait_until(tstart + TG_O);
#pragma nounroll
    for (u32 sp = 0; sp < SPIN_CAP; ++sp) {
      CBAR();
      if (!ok) va = ld_sc1(rs, gb);
      ok = ok4(va, tag);
      if (__all(ok)) break;
    }
    *(LAS f32x4*)(L + xw) = unpack4(va);
    CBAR();
    const f32x2 ml = *(const LAS f32x2*)(L + xm);
    const float o0 = uf(L[xo]), o1 = uf(L[xo + 1]), o2 = uf(L[xo + 2]);
    SCHED();
    const float f = __builtin_amdgcn_exp2f((ml[0] - max8(ml[0])) * LOG2E);
    float t0 = f * o0, t1 = f * o1, t2 = f * o2, t3 = f * ml[1];
    t0 += dpp<XOR1>(t0); t1 += dpp<XOR1>(t1); t2 += dpp<XOR1>(t2); t3 += dpp<XOR1>(t3);
    t3 += dpp<XOR2>(t3); t0 += dpp<XOR2>(t0); t1 += dpp<XOR2>(t1); t2 += dpp<XOR2>(t2);
    t3 += dpp<HMIR>(t3); t0 += dpp<HMIR>(t0); t1 += dpp<HMIR>(t1); t2 += dpp<HMIR>(t2);
    const float li = __builtin_amdgcn_rcpf(t3);
    t0 *= li; t1 *= li; t2 *= li;
    const u32 v0 = pk_bf16(t0, t0), v1 = pk_bf16(t1, t1), v2 = pk_bf16(t2, t2);
    const f32x4 zero = {0.f, 0.f, 0.f, 0.f};
    f32x4 aA = mfma(sel(v0, mA), wr[0], zero), aB = mfma(sel(v0, mB), wr[3], zero);
    aA = mfma(sel(v1, mA), wr[1], aA); aB = mfma(sel(v1, mB), wr[4], aB);
    aA = mfma(sel(v2, mA), wr[2], aA);
    aA = mfma(sel(v2, mB), wr[5], aA);
    *(LAS f32x4*)(L + wadr) = aA + aB;
    WG_BARRIER();
    if (tw < 4) {
      const f32x4 t = sum_r8(*(const LAS f32x4*)(L + radr) + *(const LAS f32x4*)(L + radr + 128));
      const u32x4 pk = pack4(t, tag);
      if (lane < 8) st_sc1(rs, sadr, pk);
    }
    return;
  }

  const int fi = (idx >> 3) * 2 + (s - 5);
  if (fi < 6) {
    const int ft = xcd * 6 + fi, mat = ft & 1, tile = ft >> 1;
    lds_wait_ge(LV + C_VISS, 4);
    u32 gw[5][8];
    {
      const rsrc_t rg = mkrs((mat ? wu_ : wg_) + (size_t)80 * tw * F + tile * 64, 79 * F * 2 + 128);
      const int goff = 10 * r8 * (F * 2) + c8 * 16;
      u32x4 t[10];
      wait_until(tstart + TG_F);
#pragma unroll
      for (int i = 0; i < 10; ++i) {
        if (i == F_EARLY) { SCHED(); lds_wait_ge(LV + C_PUB, 1); SCHED(); }
        t[i] = ld_nt(rg, goff + i * (F * 2));
        SCHED();
      }
      SCHED();
#pragma unroll
      for (int i = 0; i < 5; ++i)
#pragma unroll
        for (int d = 0; d < 4; ++d) {
          gw[i][2 * d] = __builtin_amdgcn_perm(t[2 * i + 1][d], t[2 * i][d], 0x05040100u);
          gw[i][2 * d + 1] = __builtin_amdgcn_perm(t[2 * i + 1][d], t[2 * i][d], 0x07060302u);
        }
#pragma unroll
      for (int i = 0; i < 5; ++i)
#pragma unroll
        for (int c = 0; c < 8; ++c) PIN(gw[i][c]);
    }
    int wadr = L_T + (tw * 2 + ((lane >> 3) & 1)) * 64 + c8 * 8 + (lane >> 5) * 4 + ((lane >> 4) & 1) * 2;
    int radr = L_T + (lane >> 4) * 256 + (lane & 15) * 4;
    auto gemv = [&](const u32 (&hn)[5], float (&ac)[8]) {
#pragma unroll
      for (int i = 0; i < 5; ++i)
#pragma unroll
        for (int c = 0; c < 8; ++c) ac[c] = dot2(gw[i][c], hn[i], ac[c]);
      float b4[4];
#pragma unroll
      for (int c = 0; c < 4; ++c) b4[c] = tr32(ac[c], ac[c + 4]);
      *(LAS f32x2*)(L + wadr) = f32x2{tr16(b4[0], b4[2]), tr16(b4[1], b4[3])};
    };
    auto total = [&]() -> float {
      const f32x4 g = (*(const LAS f32x4*)(L + radr) + *(const LAS f32x4*)(L + radr + 64)) + (*(const LAS f32x4*)(L + radr + 128) + *(const LAS f32x4*)(L + radr + 192));
      return tr16(tr32(g[0], g[2]), tr32(g[1], g[3]));
    };
    float ac[8];
    {
      const u32 ones[5] = {0x3f803f80u, 0x3f803f80u, 0x3f803f80u, 0x3f803f80u, 0x3f803f80u};
#pragma unroll
      for (int c = 0; c < 8; ++c) ac[c] = 0.f;
      gemv(ones, ac);
    }
    WG_BARRIER();
    float cw = 0.f;
    if (tw == 0) cw = total();
    PIN(cw);
    WG_BARRIER();
#pragma unroll
    for (int c = 0; c < 8; ++c) { ac[c] = 0.f; PIN(ac[c]); }
    float e5 = c8 < 5 ? 1.0f : 0.0f;
    int sadr = WS_GU + (lane >> 4) * GU_REP + mat * 6144 + tile * 256 + (lane & 15) * 16;
    int stw = L_ST + tw, str = L_ST + c8;
    PIN(e5); PIN(sadr); PIN(wadr); PIN(radr); PIN(stw); PIN(str);
    u32x2 hv;
    {
      const int o0 = WS_HS + xcd * HS_REP + (40 * tw + 5 * r8 + (c8 < 5 ? c8 : 0)) * 8;
#pragma nounroll
      for (u32 sp = 0; sp < SPIN_CAP; ++sp) {
        CBAR();
        hv = ld_sc1_64(rs, o0);
        if (__all(hv[1] == tag)) break;
      }
    }
    u32 hn[5];
    hn[0] = __builtin_amdgcn_ds_swizzle(hv[0], 0x18 | (0 << 5));
    hn[1] = __builtin_amdgcn_ds_swizzle(hv[0], 0x18 | (1 << 5));
    hn[2] = __builtin_amdgcn_ds_swizzle(hv[0], 0x18 | (2 << 5));
    hn[3] = __builtin_amdgcn_ds_swizzle(hv[0], 0x18 | (3 << 5));
    hn[4] = __builtin_amdgcn_ds_swizzle(hv[0], 0x18 | (4 << 5));
    const float pv = lo_f(__builtin_amdgcn_readfirstlane(hv[0]));
    const float d0 = (lo_f(hv[0]) - pv) * e5, d1 = (hi_f(hv[0]) - pv) * e5;
    float s1 = sum8(d0 + d1), s2 = sum8(d0 * d0 + d1 * d1);
    s1 += dpp<ROR8>(s1); s2 += dpp<ROR8>(s2);
    float ss = tr32(s1, s2);
    ss = tr16(ss, ss);
    gemv(hn, ac);
    {
      auto sw = __builtin_amdgcn_permlane32_swap(fu(ss), fu(ss), false, false);
      const float t1 = uf(sw[0]), t2 = uf(sw[1]);
      if (lane == 0) { L[stw] = fu(pv + t1 * (1.0f / 80.0f)); L[stw + 8] = fu(t2 - t1 * t1 * (1.0f / 80.0f)); }
    }
    WG_BARRIER();
    if (tw == 0) {
      const float mw = uf(L[str]), qw = uf(L[str + 8]);
      const float G = total();
      const float mean = sum8(mw) * 0.125f;
      const float var = rbf(sum8(qw + 80.0f * (mw - mean) * (mw - mean)) * (1.0f / D));
      const float rinv = __builtin_amdgcn_rcpf(rbf(__builtin_amdgcn_sqrtf(rbf(var + 1e-5f))));
      const float g = (G - rbf(mean) * cw) * rinv;
      auto sa = __builtin_amdgcn_permlane16_swap(fu(g), fu(g), false, false);
      const u32 pr = pk_bf16(uf(sa[0]), uf(sa[1]));
      auto sb = __builtin_amdgcn_permlane32_swap(pr, pr, false, false);
      st_sc1(rs, sadr, u32x4{sb[0], tag, sb[1], tag});
    }
  }
}

extern "C" void kernel_launch(void* const* d_in, const int* in_sizes, int n_in,
                              void* d_out, int out_size, void* d_ws, size_t ws_size,
                              hipStream_t stream) {
  hipLaunchKernelGGL(fused_block, dim3(256), dim3(NTHR), 0, stream,
                     (const u16*)d_in[0], (const u16*)d_in[1], (const u16*)d_in[2], (const u16*)d_in[3], (const u16*)d_in[4],
                     (const u16*)d_in[6], (const u16*)d_in[7], (const u16*)d_in[8], (u16*)d_out, (unsigned char*)d_ws);
}
```

```cpp
#include <hip/hip_runtime.h>
#include <hip/hip_bf16.h>
#include <stdint.h>

typedef unsigned short u16;
typedef unsigned u32;
typedef unsigned long long u64;
typedef u32 u32x4 __attribute__((ext_vector_type(4)));
typedef u32 u32x2 __attribute__((ext_vector_type(2)));
typedef float f32x4 __attribute__((ext_vector_type(4)));
typedef float f32x2 __attribute__((ext_vector_type(2)));
typedef __bf16 bf16x8 __attribute__((ext_vector_type(8)));
typedef __bf16 bf16x2 __attribute__((ext_vector_type(2)));
#define GAS __attribute__((address_space(1)))
#define LAS __attribute__((address_space(3)))

constexpr int D = 640, H = 32, DH = 192, SK = 8192, W = 2048, F = 1536, HD = H * DH;
constexpr int NTHR = 768;
constexpr float LOG2E = 1.4426950408889634f;

constexpr int WIN = 8;
constexpr int WVA = 9, WVB = 7;
constexpr int KGATE = 24;
constexpr int WOWIN = 3;
constexpr int F_EARLY = 4;
constexpr int TG_QP = 395, TG_Q = 495, TG_WO = 795, TG_F = 805, TG_D = 915, TG_P = 965, TG_O = 1035, TG_R = 1175;
constexpr int LI1 = 22, LT1 = 670, LWIN = 14, TL_WO = 840;
constexpr int PF_BYTES = 15744, PF_REP = 3, PF_T0 = 100, PF_PER = 350;

constexpr int WS_EPOCH = 0;
constexpr int HS_REP = 2560;
constexpr int WS_HS = 4096;
constexpr int GU_REP = 12288;
constexpr int WS_GU = 32768;
constexpr int WS_YP = WS_GU + 4 * GU_REP;
constexpr int WS_QP = WS_YP + H * D * 4;
constexpr int OP_REC = 1024;
constexpr int WS_OP = WS_QP + H * 8 * DH * 4;
constexpr int WS_END = WS_OP + H * 8 * OP_REC;
constexpr u32 SPIN_CAP = 1u << 21;

constexpr int C_Q = 0, C_KISS = 1, C_VISS = 2, C_O = 3, C_P = 4, C_PUB = 5, C_TAG = 6, C_PF = 8;
constexpr int L_Q = 16;
constexpr int L_P = 112;
constexpr int L_ML = L_P + 384;
constexpr int L_O = L_ML + 8;
constexpr int L_T = L_O + 8 * 192;
constexpr int L_ST = L_T + 2048;
constexpr int L_X = L_ST + 16;
constexpr int L_END = L_X + 8 * 288;

__device__ __forceinline__ u32 fu(float f) { return __float_as_uint(f); }
__device__ __forceinline__ float uf(u32 u) { return __uint_as_float(u); }
__device__ __forceinline__ float lo_f(u32 u) { return uf(u << 16); }
__device__ __forceinline__ float hi_f(u32 u) { return uf(u & 0xffff0000u); }
__device__ __forceinline__ u32 pk_bf16(float lo, float hi) {
  bf16x2 v = {(__bf16)lo, (__bf16)hi};
  return __builtin_bit_cast(u32, v);
}
__device__ __forceinline__ float rbf(float f) { return (float)(__bf16)f; }
__device__ __forceinline__ float dot2(u32 a, u32 b, float acc) {
  return __builtin_amdgcn_fdot2_f32_bf16(__builtin_bit_cast(bf16x2, a), __builtin_bit_cast(bf16x2, b), acc, false);
}
__device__ __forceinline__ f32x4 mfma(u32x4 a, u32x4 b, f32x4 c) {
  return __builtin_amdgcn_mfma_f32_16x16x32_bf16(__builtin_bit_cast(bf16x8, a), __builtin_bit_cast(bf16x8, b), c, 0, 0, 0);
}
__device__ __forceinline__ u32x4 sel(u32 v, u32x4 m) { return u32x4{v & m[0], v & m[1], v & m[2], v & m[3]}; }
template <int CTRL> __device__ __forceinline__ float dpp(float x) {
  return __builtin_bit_cast(float, __builtin_amdgcn_mov_dpp(__builtin_bit_cast(int, x), CTRL, 0xf, 0xf, true));
}
template <int CTRL> __device__ __forceinline__ u32 dppu(u32 x) { return (u32)__builtin_amdgcn_mov_dpp((int)x, CTRL, 0xf, 0xf, true); }
constexpr int XOR1 = 0xB1, XOR2 = 0x4E, HMIR = 0x141, ROR8 = 0x128;
__device__ __forceinline__ float sum8(float x) { x += dpp<XOR1>(x); x += dpp<XOR2>(x); x += dpp<HMIR>(x); return x; }
__device__ __forceinline__ float max8(float x) {
  float r;
  asm volatile("s_nop 1\n\tv_max_f32_dpp %0, %1, %1 quad_perm:[1,0,3,2] row_mask:0xf bank_mask:0xf\n\t"
               "s_nop 1\n\tv_max_f32_dpp %0, %0, %0 quad_perm:[2,3,0,1] row_mask:0xf bank_mask:0xf\n\t"
               "s_nop 1\n\tv_max_f32_dpp %0, %0, %0 row_half_mirror row_mask:0xf bank_mask:0xf" : "=&v"(r) : "v"(x));
  return r;
}
__device__ __forceinline__ float max16(float x) {
  float r;
  asm volatile("s_nop 1\n\tv_max_f32_dpp %0, %1, %1 quad_perm:[1,0,3,2] row_mask:0xf bank_mask:0xf\n\t"
               "s_nop 1\n\tv_max_f32_dpp %0, %0, %0 quad_perm:[2,3,0,1] row_mask:0xf bank_mask:0xf\n\t"
               "s_nop 1\n\tv_max_f32_dpp %0, %0, %0 row_half_mirror row_mask:0xf bank_mask:0xf\n\t"
               "s_nop 1\n\tv_max_f32_dpp %0, %0, %0 row_ror:8 row_mask:0xf bank_mask:0xf\n\ts_nop 1" : "=&v"(r) : "v"(x));
  return r;
}
__device__ __forceinline__ float tr32(float a, float b) {
  auto t = __builtin_amdgcn_permlane32_swap(fu(a), fu(b), false, false);
  return uf(t[0]) + uf(t[1]);
}
__device__ __forceinline__ float tr16(float a, float b) {
  auto t = __builtin_amdgcn_permlane16_swap(fu(a), fu(b), false, false);
  return uf(t[0]) + uf(t[1]);
}
__device__ __forceinline__ float xrow_sum(float x) { x = tr16(x, x); return tr32(x, x); }
__device__ __forceinline__ float xrow_max(float x) {
  auto s = __builtin_amdgcn_permlane16_swap(fu(x), fu(x), false, false);
  x = fmaxf(uf(s[0]), uf(s[1]));
  auto t = __builtin_amdgcn_permlane32_swap(fu(x), fu(x), false, false);
  return fmaxf(uf(t[0]), uf(t[1]));
}
__device__ __forceinline__ float sum64(float x) { x = sum8(x); x += dpp<ROR8>(x); return xrow_sum(x); }
__device__ __forceinline__ f32x4 sum_r8(f32x4 t) {
  float w = tr16(tr32(t[0], t[1]), tr32(t[2], t[3]));
  w += dpp<ROR8>(w);
  auto sa = __builtin_amdgcn_permlane16_swap(fu(w), fu(w), false, false);
  auto se = __builtin_amdgcn_permlane32_swap(sa[0], sa[0], false, false);
  auto so = __builtin_amdgcn_permlane32_swap(sa[1], sa[1], false, false);
  return f32x4{uf(se[0]), uf(se[1]), uf(so[0]), uf(so[1])};
}

typedef __amdgpu_buffer_rsrc_t rsrc_t;
__device__ __forceinline__ rsrc_t mkrs(const void* p, int bytes) { return __builtin_amdgcn_make_buffer_rsrc((void*)p, 0, bytes, 0x00020000); }
__device__ __forceinline__ u32x4 ld_nt(rsrc_t rs, int off) { return __builtin_amdgcn_raw_buffer_load_b128(rs, off, 0, 2); }
__device__ __forceinline__ u32x4 ld_sc1(rsrc_t rs, int off) { return __builtin_amdgcn_raw_buffer_load_b128(rs, off, 0, 16); }
__device__ __forceinline__ u32x2 ld_sc1_64(rsrc_t rs, int off) { return __builtin_amdgcn_raw_buffer_load_b64(rs, off, 0, 16); }
__device__ __forceinline__ void st_sc1(rsrc_t rs, int off, u32x4 v) { __builtin_amdgcn_raw_buffer_store_b128(v, rs, off, 0, 16); }
template <int N0, int N1, int N2> __device__ __forceinline__ void vmw3(int mode) {
  asm volatile("s_cmp_eq_u32 %0, 2\n\ts_cbranch_scc1 1f\n\ts_waitcnt vmcnt(%2)\n\ts_cmp_eq_u32 %0, 1\n\ts_cbranch_scc1 1f\n\t"
               "s_waitcnt vmcnt(%1)\n1:\n\ts_waitcnt vmcnt(%3)" :: "s"(mode), "i"(N0), "i"(N1), "i"(N2) : "memory", "scc");
}
template <int N> __device__ __forceinline__ void vmw() { asm volatile("s_waitcnt vmcnt(%0)" :: "i"(N) : "memory"); }

__device__ __forceinline__ u32x4 pack4(f32x4 v, u32 tag) {
  const u32 a = fu(v[0]) + 0x80u, b = fu(v[1]) + 0x80u, c = fu(v[2]) + 0x80u, d = fu(v[3]) + 0x80u;
  return u32x4{__builtin_amdgcn_perm(a, b, 0x07060503u), __builtin_amdgcn_perm(b, tag, 0x06050100u),
               __builtin_amdgcn_perm(c, d, 0x07060503u), __builtin_amdgcn_perm(d, tag, 0x06050100u)};
}
__device__ __forceinline__ f32x4 unpack4(u32x4 w) {
  return f32x4{uf(w[0] & 0xffffff00u), uf(__builtin_amdgcn_perm(w[0], w[1], 0x0403020cu)),
               uf(w[2] & 0xffffff00u), uf(__builtin_amdgcn_perm(w[2], w[3], 0x0403020cu))};
}
__device__ __forceinline__ u32x4 packq(f32x4 v, u32 tag) {
  const u32 e = max(max(fu(v[0]) & 0x7fffffffu, fu(v[1]) & 0x7fffffffu), max(fu(v[2]) & 0x7fffffffu, fu(v[3]) & 0x7fffffffu)) >> 23;
  u32 m[4];
#pragma unroll
  for (int i = 0; i < 4; ++i) m[i] = (u32)min(max((int)__builtin_rintf(__builtin_amdgcn_ldexpf(v[i], 147 - (int)e)), -2097151), 2097151);
  const u32 t = tag & 0xffffu;
  return u32x4{(m[0] << 10) | ((m[1] >> 12) & 0x3ffu), (m[1] << 20) | ((e & 15u) << 16) | t,
               (m[2] << 10) | ((m[3] >> 12) & 0x3ffu), (m[3] << 20) | ((e >> 4) << 16) | t};
}
__device__ __forceinline__ f32x4 unpackq(u32x4 w) {
  const int e = (int)(((w[1] >> 16) & 15u) | ((w[3] >> 12) & 0xf0u));
  const int m[4] = {(int)w[0] >> 10, (int)__builtin_amdgcn_alignbit(w[0], w[1], 10) >> 10, (int)w[2] >> 10, (int)__builtin_amdgcn_alignbit(w[2], w[3], 10) >> 10};
  f32x4 r;
#pragma unroll
  for (int i = 0; i < 4; ++i) r[i] = e == 255 ? __builtin_nanf("") : __builtin_amdgcn_ldexpf((float)m[i], e - 147);
  return r;
}
__device__ __forceinline__ bool ok4(u32x4 w, u32 tag) { return (((w[1] ^ tag) | (w[3] ^ tag)) & 0xffffu) == 0u; }

#define CBAR() asm volatile("" ::: "memory")
#define PIN(x) asm volatile("" : "+v"(x))
#define LGKM0() asm volatile("s_waitcnt lgkmcnt(0)" ::: "memory")
#define WG_BARRIER() asm volatile("s_waitcnt lgkmcnt(0)\n\ts_barrier" ::: "memory")
#define SCHED() __builtin_amdgcn_sched_barrier(0)

__device__ __forceinline__ void lds_wait_ge(volatile LAS u32* w, u32 n) {
#pragma nounroll
  for (u32 sp = 0; sp < SPIN_CAP; ++sp) {
    if (*w >= n) break;
    __builtin_amdgcn_s_sleep(1);
  }
  CBAR();
}
__device__ __forceinline__ void wait_until(u64 t) {
#pragma nounroll
  for (u32 sp = 0; sp < 4096; ++sp) {
    if (__builtin_amdgcn_s_memrealtime() >= t) break;
    __builtin_amdgcn_s_sleep(1);
  }
  CBAR();
}
__device__ __forceinline__ void lds_inc(LAS u32* w, int lane) {
  CBAR();
  if (lane == 0) __hip_atomic_fetch_add(w, 1u, __ATOMIC_RELAXED, __HIP_MEMORY_SCOPE_WORKGROUP);
  CBAR();
}

__global__ void __launch_bounds__(NTHR, 3)
fused_block(const u16* __restrict__ x_, const u16* __restrict__ kc_, const u16* __restrict__ vc_, const u16* __restrict__ wq_,
            const u16* __restrict__ wo_, const u16* __restrict__ wg_, const u16* __restrict__ wu_, const u16* __restrict__ wd_,
            u16* __restrict__ out_, unsigned char* ws_) {
  u64 pc0; asm volatile("s_getpc_b64 %0" : "=s"(pc0));
  __shared__ __attribute__((aligned(16))) u32 smem[L_END];
  LAS u32* L = (LAS u32*)smem;
  volatile LAS u32* LV = (volatile LAS u32*)smem;
  const int tid = threadIdx.x, lane = tid & 63, wave = __builtin_amdgcn_readfirstlane(tid >> 6);
  const int b = blockIdx.x;
  const int xcd = b & 7, idx = b >> 3;
  const int h = xcd * 4 + (idx >> 3), s = idx & 7;
  rsrc_t rs;
  const int r8 = lane >> 3, c8 = lane & 7;
  u64 tstart;
  u32x4 msk;
#pragma unroll
  for (int d = 0; d < 4; ++d) msk[d] = (d == (c8 >> 1)) ? ((c8 & 1) ? 0xffff0000u : 0x0000ffffu) : 0u;
  u32 tag;
#define START() do { unsigned char* w_ = ws_; asm volatile("" : "+s"(w_) : "s"(x_), "s"(kc_), "s"(vc_), "s"(wq_), "s"(wo_), "s"(wg_), "s"(wu_), "s"(wd_), "s"(out_)); \
    rs = mkrs(w_, WS_END); tstart = __builtin_amdgcn_s_memrealtime(); SCHED(); } while (0)
#define FIRST_BARRIER() do { WG_BARRIER(); tag = __builtin_amdgcn_readfirstlane(LV[C_TAG]); } while (0)
  const int g0 = 36 * s + (s > 3 ? s - 3 : 0), ng = s >= 3 ? 37 : 36;
  const size_t kvoff = ((size_t)h * SK + (SK - W) + g0 * 8) * DH;

  if (wave < 8 && s == 7) {
    START();
    FIRST_BARRIER();
    if (wave == 2 && idx >= 16) {
      const rsrc_t rc = mkrs((const void*)(pc0 & ~127ull), PF_BYTES);
      const int off = ((3 - (idx >> 3)) * 64 + lane) * 128;
#pragma nounroll
      for (int r = 0; r < PF_REP; ++r) {
        wait_until(tstart + PF_T0 + r * PF_PER);
        u32 t1 = __builtin_amdgcn_raw_buffer_load_b32(rc, off, 0, 2);
        asm volatile("" :: "v"(t1));
      }
    }
  } else if (wave < 4) {
    __builtin_amdgcn_s_setprio(2);
    START();
    const rsrc_t rk = mkrs(kc_ + kvoff, ng * 3072);
    const int loff = wave * 3072 + r8 * 384 + c8 * 16;
    u32x4 kr[30];
#pragma unroll
    for (int i = 0; i < 30; ++i) {
      if (i == WIN) FIRST_BARRIER();
      if (i >= WIN) vmw<WIN - 1>();
      if (i == KGATE) lds_inc(L + C_KISS, lane);
      kr[i] = ld_nt(rk, loff + (i / 3) * 12288 + (i % 3) * 128);
      SCHED();
    }
    float dd[10];
    const bool sbok = c8 == 0 || (c8 == 1 && 36 + wave < ng);
    int padr = L_P + wave * 96 + r8 * 12 + c8, padr2 = c8 < 2 ? padr + 8 : L_T + lane;
    PIN(padr); PIN(padr2);
#pragma unroll
    for (int g = 0; g < 10; ++g) { dd[g] = 0.f; PIN(dd[g]); }
    lds_wait_ge(LV + C_Q, 3);
    u32x4 qp[3];
#pragma unroll
    for (int t = 0; t < 3; ++t) qp[t] = *(const LAS u32x4*)(L + L_Q + t * 32 + c8 * 4);
#pragma unroll
    for (int e = 0; e < 12; ++e) {
#pragma unroll
      for (int g = 0; g < 10; ++g) dd[g] = dot2(kr[g * 3 + e / 4][e % 4], qp[e / 4][e % 4], dd[g]);
      SCHED();
    }
    const bool b0 = c8 & 1, b1 = c8 & 2, b2 = c8 & 4;
    float e4[4], e2[2];
#pragma unroll
    for (int i = 0; i < 4; ++i) { const float keep = b2 ? dd[i + 4] : dd[i], give = b2 ? dd[i] : dd[i + 4]; e4[i] = keep + dpp<HMIR>(give); }
#pragma unroll
    for (int i = 0; i < 2; ++i) { const float keep = b1 ? e4[i + 2] : e4[i], give = b1 ? e4[i] : e4[i + 2]; e2[i] = keep + dpp<XOR2>(give); }
    float sa = (b0 ? e2[1] : e2[0]) + dpp<XOR1>(b0 ? e2[0] : e2[1]);
    float sb = (b0 ? dd[9] : dd[8]) + dpp<XOR1>(b0 ? dd[8] : dd[9]);
    sb += dpp<XOR2>(sb);
    sb += dpp<0x1B>(dpp<HMIR>(sb));
    sa = rbf(rbf(sa) * 0.07216878364870323f);
    sb = rbf(rbf(sb) * 0.07216878364870323f);
    sb = sbok ? sb : -INFINITY;
    const float m = xrow_max(max16(fmaxf(sa, sb)));
    const float pa = __builtin_amdgcn_exp2f((sa - m) * LOG2E), pb = __builtin_amdgcn_exp2f((sb - m) * LOG2E);
    L[padr] = pk_bf16(pa, pa);
    L[padr2] = pk_bf16(pb, pb);
    CBAR();
    if (lane == 0) LV[C_PF + wave] = 1u;
    const float l = sum64(pa + pb);
    if (lane == 0) { L[L_ML + wave] = fu(m); L[L_ML + 4 + wave] = fu(l); }
    lds_inc(L + C_P, lane);
  } else if (wave < 8) {
    __builtin_amdgcn_s_setprio(1);
    START();
    const int w = wave - 4;
    const rsrc_t rv = mkrs(vc_ + kvoff, ng * 3072);
    const int loff = w * 3072 + r8 * 384 + c8 * 16;
    FIRST_BARRIER();
    lds_wait_ge(LV + C_KISS, 4);
    u32x4 vr[30];
    int mode = __builtin_amdgcn_readfirstlane(s < 5 ? 1 : 0);
#pragma unroll
    for (int i = 0; i < 30; ++i) {
      const int j = (i + 27) % 30;
      if (i == LI1) mode = __builtin_amdgcn_readfirstlane(__builtin_amdgcn_s_memrealtime() > tstart + LT1 ? 2 : mode);
      if (i >= WVB) vmw3<WVB - 1, WVA - 1, LWIN - 1>(mode);
      if (i == 29) lds_inc(L + C_VISS, lane);
      vr[j] = ld_nt(rv, loff + (j / 3) * 12288 + (j % 3) * 128);
      SCHED();
    }
    int wadr = ((lane >> 5) == ((lane >> 3) & 1) ? L_O + (w * 2 + (lane >> 5)) * 192 : L_T + 64 + w * 192) + c8 * 8 + ((lane >> 4) & 1) * 4;
    int radr = L_O + 24 * r8 + 4 * (c8 < 6 ? c8 : 0);
    int sadr = WS_OP + (h * 8 + s) * OP_REC + lane * 16;
    u32 tadr = (u32)(uintptr_t)(L + C_O), one = 1u;
    PIN(wadr); PIN(radr); PIN(sadr); PIN(tadr); PIN(one);
    lds_wait_ge(LV + C_PF + w, 1);
    const LAS u32* pr = L + L_P + w * 96 + r8 * 12;
    const u32x4 p0 = *(const LAS u32x4*)(pr), p1 = *(const LAS u32x4*)(pr + 4);
    const u32x2 p2 = *(const LAS u32x2*)(pr + 8);
    float M, Ls, fw;
    f32x4 acc[2][3];
#pragma unroll
    for (int gg = 0; gg < 10; ++gg) {
      const int g = (gg + 9) % 10;
      if (gg == 5) {
        SCHED();
        lds_wait_ge(LV + C_P, 4);
        const f32x4 mm = *(const LAS f32x4*)(L + L_ML), ll = *(const LAS f32x4*)(L + L_ML + 4);
        M = fmaxf(fmaxf(mm[0], mm[1]), fmaxf(mm[2], mm[3]));
        f32x4 ff;
#pragma unroll
        for (int k = 0; k < 4; ++k) ff[k] = __builtin_amdgcn_exp2f((mm[k] - M) * LOG2E);
        Ls = (ff[0] * ll[0] + ff[1] * ll[1]) + (ff[2] * ll[2] + ff[3] * ll[3]);
        fw = w == 0 ? ff[0] : (w == 1 ? ff[1] : (w == 2 ? ff[2] : ff[3]));
        PIN(M); PIN(Ls); PIN(fw);
      }
      const u32x4 A = sel(g < 4 ? p0[g & 3] : (g < 8 ? p1[g & 3] : p2[g & 1]), msk);
#pragma unroll
      for (int t = 0; t < 3; ++t) {
        if (gg == 9) acc[0][t] = mfma(A, vr[g * 3 + t], acc[0][t] + acc[1][t]);
        else acc[gg & 1][t] = mfma(A, vr[g * 3 + t], gg < 2 ? f32x4{0.f, 0.f, 0.f, 0.f} : acc[gg & 1][t]);
      }
    }
#pragma unroll
    for (int t = 0; t < 3; ++t) *(LAS f32x4*)(L + wadr + t * 64) = acc[0][t] * fw;
    u32 old;
    asm volatile("s_mov_b64 exec, 1\n\tds_add_rtn_u32 %0, %1, %2\n\ts_mov_b64 exec, -1\n\ts_waitcnt lgkmcnt(0)" : "=&v"(old) : "v"(tadr), "v"(one) : "memory");
    if (__builtin_amdgcn_readfirstlane(old) == 3) {
      const u64 tnow = __builtin_amdgcn_s_memrealtime();
      f32x4 pt[8];
#pragma unroll
      for (int k = 0; k < 8; ++k) pt[k] = *(const LAS f32x4*)(L + radr + k * 192);
      SCHED();
      f32x4 o = ((pt[0] + pt[1]) + (pt[2] + pt[3])) + ((pt[4] + pt[5]) + (pt[6] + pt[7]));
      if (c8 == 6) { o[0] = M; o[1] = Ls; }
      u32x4 pk = pack4(o, tag);
      PIN(pk);
      if (tnow < tstart + TG_P) wait_until(tstart + TG_P);
      st_sc1(rs, sadr, pk);
      if (lane == 0) LV[C_PUB] = 1u;
    }
    return;
  } else {
    const int a = wave - 8;
    __builtin_amdgcn_s_setprio(3);
    START();
    if (a < 3) {
      const GAS char* xb = (const GAS char*)x_;
      const u32x4 xa = *(const GAS u32x4*)(xb + lane * 16);
      const u32 xc = *(const GAS u32*)(xb + 1024 + lane * 4);
      const GAS u32* xr = (const GAS u32*)(xb + 160 * s + 20 * r8);
      u32 xo[5];
#pragma unroll
      for (int i = 0; i < 5; ++i) xo[i] = xr[i];
      const rsrc_t rq = mkrs(wq_ + (size_t)80 * s * HD + h * DH + a * 64, 79 * HD * 2 + 128);
      const int qoff = 10 * r8 * (HD * 2) + c8 * 16;
      u32x4 wr[10];
#pragma unroll
      for (int g = 0; g < 10; ++g) wr[g] = ld_nt(rq, qoff + g * (HD * 2));
      SCHED();
      FIRST_BARRIER();
      SCHED();
      const float xv[10] = {lo_f(xa[0]), hi_f(xa[0]), lo_f(xa[1]), hi_f(xa[1]), lo_f(xa[2]), hi_f(xa[2]), lo_f(xa[3]), hi_f(xa[3]), lo_f(xc), hi_f(xc)};
      float sm = 0.f;
#pragma unroll
      for (int i = 0; i < 10; ++i) sm += xv[i];
      const float mean = sum64(sm) * (1.0f / D);
      float sq = 0.f;
#pragma unroll
      for (int i = 0; i < 10; ++i) { const float dd = xv[i] - mean; sq += dd * dd; }
      const float var = rbf(sum64(sq) * (1.0f / D));
      const float mb = rbf(mean);
      const float rinv = 1.0f / rbf(__builtin_sqrtf(rbf(var + 1e-5f)));
      f32x4 acc = {0.f, 0.f, 0.f, 0.f};
#pragma unroll
      for (int g = 0; g < 10; ++g) {
        const float xg = (g & 1) ? hi_f(xo[g >> 1]) : lo_f(xo[g >> 1]);
        const float xn = rbf(xg - mb) * rinv;
        acc = mfma(sel(pk_bf16(xn, xn), msk), wr[g], acc);
      }
#pragma unroll
      for (int i = 0; i < 4; ++i) {
        const float up = dpp<ROR8>(acc[i]);
        auto t = __builtin_amdgcn_permlane32_swap(fu(up), fu(up), false, false);
        acc[i] += uf(t[1]);
      }
      u32x4 pk = packq(acc, tag);
      PIN(pk);
      wait_until(tstart + TG_QP);
      if (lane < 32 && (lane & 8) == 0) st_sc1(rs, WS_QP + (h * 8 + s) * (DH * 4) + a * 256 + (c8 + 8 * (lane >> 4)) * 16, pk);
      if (s < 7) {
        const int gb = WS_QP + (h * 8 + (lane >> 4)) * (DH * 4) + a * 256 + (lane & 15) * 16;
        u32x4 v[2];
        bool ok[2] = {false, false};
        wait_until(tstart + TG_Q);
#pragma nounroll
        for (u32 sp = 0; sp < SPIN_CAP; ++sp) {
          CBAR();
#pragma unroll
          for (int i = 0; i < 2; ++i) if (!ok[i]) v[i] = ld_sc1(rs, gb + i * (4 * DH * 4));
#pragma unroll
          for (int i = 0; i < 2; ++i) ok[i] = ok4(v[i], tag);
          if (__all(ok[0] & ok[1])) break;
          __builtin_amdgcn_s_sleep(2);
        }
        f32x4 q = unpackq(v[0]) + unpackq(v[1]);
#pragma unroll
        for (int i = 0; i < 4; ++i) q[i] = xrow_sum(q[i]);
        if (lane < 16) *(LAS u32x2*)(L + L_Q + a * 32 + 4 * c8 + 2 * (lane >> 3)) = u32x2{pk_bf16(q[0], q[1]), pk_bf16(q[2], q[3])};
        lds_inc(L + C_Q, lane);
      }
    } else {
      u32 epw;
      asm volatile("s_load_dword %0, %1, 0x0\n\ts_waitcnt lgkmcnt(0)" : "=&s"(epw) : "s"(ws_) : "memory");
      if (lane < 16) L[lane] = lane == C_TAG ? epw + 1u : 0u;
      FIRST_BARRIER();
    }
  }
  __builtin_amdgcn_s_setprio(0);

  if (s == 7) {
    const int j4 = idx >> 3;
    const int rI = j4 < 2 ? 2 * xcd + j4 : 14 + 2 * xcd + j4;
    if (rI < 20) {
      const int dt = rI;
      const int r16 = lane >> 2, c4 = lane & 3;
      u32 dw[4][8];
      {
        const rsrc_t rd = mkrs(wd_ + (size_t)128 * wave * D + dt * 32, 127 * D * 2 + 64);
        const int doff = 8 * r16 * (D * 2) + c4 * 16;
        wait_until(tstart + TG_D);
        u32x4 t[8];
#pragma unroll
        for (int i = 0; i < 8; ++i) { t[i] = ld_nt(rd, doff + i * (D * 2)); SCHED(); }
#pragma unroll
        for (int k = 0; k < 4; ++k)
#pragma unroll
          for (int d = 0; d < 4; ++d) {
            dw[k][2 * d] = __builtin_amdgcn_perm(t[2 * k + 1][d], t[2 * k][d], 0x05040100u);
            dw[k][2 * d + 1] = __builtin_amdgcn_perm(t[2 * k + 1][d], t[2 * k][d], 0x07060302u);
          }
#pragma unroll
        for (int k = 0; k < 4; ++k)
#pragma unroll
          for (int c = 0; c < 8; ++c) PIN(dw[k][c]);
      }
      float ac[8];
#pragma unroll
      for (int c = 0; c < 8; ++c) { ac[c] = 0.f; PIN(ac[c]); }
      int wadr = L_T + (wave * 4 + ((lane >> 2) & 3)) * 32 + c4 * 8 + (lane >> 5) * 4 + ((lane >> 4) & 1) * 2;
      int radr = L_T + r8 * 192 + c8 * 4;
      int oadr = dt * 64 + c8 * 8;
      PIN(wadr); PIN(radr); PIN(oadr);
      const rsrc_t ro = mkrs(out_, D * 2);
      u32x2 gv, uv;
      u32x4 hv = {0u, tag, 0u, tag};
      {
        const int o0 = WS_GU + ((rI >> 1) & 3) * GU_REP + (64 * wave + lane) * 8;
        const int oh = WS_HS + xcd * HS_REP + (dt * 16 + c8 * 2) * 8;
#pragma nounroll
        for (u32 sp = 0; sp < SPIN_CAP; ++sp) {
          CBAR();
          gv = ld_sc1_64(rs, o0); uv = ld_sc1_64(rs, o0 + 6144);
          if (wave == 0) hv = ld_sc1(rs, oh);
          if (__all(((gv[1] ^ tag) | (uv[1] ^ tag) | (hv[1] ^ tag) | (hv[3] ^ tag)) == 0u)) break;
        }
      }
      auto glu = [](float g, float u) -> float { return rbf(__builtin_amdgcn_rcpf(1.0f + __builtin_amdgcn_exp2f(-g * LOG2E))) * u; };
      const u32 ha = pk_bf16(glu(lo_f(gv[0]), lo_f(uv[0])), glu(hi_f(gv[0]), hi_f(uv[0])));
      const u32 hh[4] = {dppu<0x00>(ha), dppu<0x55>(ha), dppu<0xAA>(ha), dppu<0xFF>(ha)};
#pragma unroll
      for (int k = 0; k < 4; ++k)
#pragma unroll
        for (int c = 0; c < 8; ++c) ac[c] = dot2(dw[k][c], hh[k], ac[c]);
      float b4[4];
#pragma unroll
      for (int c = 0; c < 4; ++c) b4[c] = tr32(ac[c], ac[c + 4]);
      *(LAS f32x2*)(L + wadr) = f32x2{tr16(b4[0], b4[2]), tr16(b4[1], b4[3])};
      WG_BARRIER();
      if (wave == 0) {
        f32x4 p[6];
#pragma unroll
        for (int k = 0; k < 6; ++k) p[k] = *(const LAS f32x4*)(L + radr + k * 32);
        const f32x4 t = sum_r8(((p[0] + p[1]) + (p[2] + p[3])) + (p[4] + p[5]));
        const u32 d0 = pk_bf16(t[0], t[1]), d1 = pk_bf16(t[2], t[3]);
        const u32x2 ov = {pk_bf16(lo_f(hv[0]) + lo_f(d0), hi_f(hv[0]) + hi_f(d0)), pk_bf16(lo_f(hv[2]) + lo_f(d1), hi_f(hv[2]) + hi_f(d1))};
        if (lane < 8) __builtin_amdgcn_raw_buffer_store_b64(ov, ro, oadr, 0, 16);
        if (rI == 0 && lane == 0) __builtin_amdgcn_raw_buffer_store_b32(tag, rs, WS_EPOCH, 0, 16);
      }
    } else if (rI < 30 && wave < 2) {
      const int ru = (rI - 20) * 2 + wave;
      const u32x2 xp = *(const GAS u32x2*)((const GAS char*)x_ + (ru * 32 + c8 * 4) * 2);
      f32x4 xf = {lo_f(xp[0]), hi_f(xp[0]), lo_f(xp[1]), hi_f(xp[1])};
      int sadr = WS_HS + r8 * HS_REP + (ru * 16 + c8 * 2) * 8;
      PIN(xf); PIN(sadr);
      const int gb = WS_YP + (r8 * D + ru * 32 + c8 * 4) * 4;
      u32x4 v[4];
      bool ok[4] = {false, false, false, false};
      wait_until(tstart + TG_R);
#pragma nounroll
      for (u32 sp = 0; sp < SPIN_CAP; ++sp) {
        CBAR();
#pragma unroll
        for (int i = 0; i < 4; ++i) if (!ok[i]) v[i] = ld_sc1(rs, gb + i * (8 * D * 4));
#pragma unroll
        for (int i = 0; i < 4; ++i) ok[i] = ok4(v[i], tag);
        if (__all(ok[0] & ok[1] & ok[2] & ok[3])) break;
      }
      const f32x4 y = sum_r8((unpack4(v[0]) + unpack4(v[1])) + (unpack4(v[2]) + unpack4(v[3])));
      const u32 a0 = pk_bf16(y[0], y[1]), a1 = pk_bf16(y[2], y[3]);
      st_sc1(rs, sadr, u32x4{pk_bf16(xf[0] + lo_f(a0), xf[1] + hi_f(a0)), tag, pk_bf16(xf[2] + lo_f(a1), xf[3] + hi_f(a1)), tag});
    }
    return;
  }

  const int tw = wave < 4 ? wave : wave - 4;
  if (s < 5) {
    lds_wait_ge(LV + C_VISS, 4);
    const int g4 = lane >> 4, z = lane & 15;
    const rsrc_t ro = mkrs(wo_ + (size_t)(h * DH + 24 * tw) * D + s * 128, 23 * D * 2 + 256);
    const int ooff = 6 * g4 * (D * 2) + z * 16;
    u32x4 wr[6];
    wait_until(tstart + TG_WO);
    const bool paced = __builtin_amdgcn_s_memrealtime() < tstart + TL_WO;
#pragma unroll
    for (int k = 0; k < 6; ++k) { if (k >= WOWIN && paced) vmw<WOWIN - 1>(); wr[k] = ld_nt(ro, ooff + k * (D * 2)); SCHED(); }
    u32x4 mA, mB;
#pragma unroll
    for (int d = 0; d < 4; ++d) { mA[d] = z < 8 ? msk[d] : 0u; mB[d] = z < 8 ? 0u : msk[d]; }
    int xw = L_X + tw * 288 + r8 * 36 + c8 * 4, xm = L_X + tw * 288 + c8 * 36 + 24, xo = L_X + tw * 288 + c8 * 36 + 3 * r8;
    int wadr = L_T + (tw * 2 + (g4 >> 1)) * 128 + z * 8 + (g4 & 1) * 4;
    int radr = L_T + r8 * 256 + tw * 32 + c8 * 4;
    int sadr = WS_YP + (h * D + s * 128 + tw * 32 + c8 * 4) * 4;
    PIN(mA); PIN(mB); PIN(xw); PIN(xm); PIN(xo); PIN(wadr); PIN(radr); PIN(sadr);
    lds_wait_ge(LV + C_PUB, 1);
    const int gb = WS_OP + (h * 8 + r8) * OP_REC + tw * 128 + c8 * 16;
    u32x4 va = {c8 == 6 ? 0xff800000u : 0u, tag & 0xffffu, 0u, tag & 0xffffu};
    bool ok = r8 == 7;
    wait_until(tstart + TG_O);
#pragma nounroll
    for (u32 sp = 0; sp < SPIN_CAP; ++sp) {
      CBAR();
      if (!ok) va = ld_sc1(rs, gb);
      ok = ok4(va, tag);
      if (__all(ok)) break;
    }
    *(LAS f32x4*)(L + xw) = unpack4(va);
    CBAR();
    const f32x2 ml = *(const LAS f32x2*)(L + xm);
    const float o0 = uf(L[xo]), o1 = uf(L[xo + 1]), o2 = uf(L[xo + 2]);
    SCHED();
    const float f = __builtin_amdgcn_exp2f((ml[0] - max8(ml[0])) * LOG2E);
    float t0 = f * o0, t1 = f * o1, t2 = f * o2, t3 = f * ml[1];
    t0 += dpp<XOR1>(t0); t1 += dpp<XOR1>(t1); t2 += dpp<XOR1>(t2); t3 += dpp<XOR1>(t3);
    t3 += dpp<XOR2>(t3); t0 += dpp<XOR2>(t0); t1 += dpp<XOR2>(t1); t2 += dpp<XOR2>(t2);
    t3 += dpp<HMIR>(t3); t0 += dpp<HMIR>(t0); t1 += dpp<HMIR>(t1); t2 += dpp<HMIR>(t2);
    const float li = __builtin_amdgcn_rcpf(t3);
    t0 *= li; t1 *= li; t2 *= li;
    const u32 v0 = pk_bf16(t0, t0), v1 = pk_bf16(t1, t1), v2 = pk_bf16(t2, t2);
    const f32x4 zero = {0.f, 0.f, 0.f, 0.f};
    f32x4 aA = mfma(sel(v0, mA), wr[0], zero), aB = mfma(sel(v0, mB), wr[3], zero);
    aA = mfma(sel(v1, mA), wr[1], aA); aB = mfma(sel(v1, mB), wr[4], aB);
    aA = mfma(sel(v2, mA), wr[2], aA);
    aA = mfma(sel(v2, mB), wr[5], aA);
    *(LAS f32x4*)(L + wadr) = aA + aB;
    WG_BARRIER();
    if (tw < 4) {
      const f32x4 t = sum_r8(*(const LAS f32x4*)(L + radr) + *(const LAS f32x4*)(L + radr + 128));
      const u32x4 pk = pack4(t, tag);
      if (lane < 8) st_sc1(rs, sadr, pk);
    }
    return;
  }

  const int fi = (idx >> 3) * 2 + (s - 5);
  if (fi < 6) {
    const int ft = xcd * 6 + fi, mat = ft & 1, tile = ft >> 1;
    lds_wait_ge(LV + C_VISS, 4);
    u32 gw[5][8];
    {
      const rsrc_t rg = mkrs((mat ? wu_ : wg_) + (size_t)80 * tw * F + tile * 64, 79 * F * 2 + 128);
      const int goff = 10 * r8 * (F * 2) + c8 * 16;
      u32x4 t[10];
      wait_until(tstart + TG_F);
#pragma unroll
      for (int i = 0; i < 10; ++i) {
        if (i == F_EARLY) { SCHED(); lds_wait_ge(LV + C_PUB, 1); SCHED(); }
        t[i] = ld_nt(rg, goff + i * (F * 2));
        SCHED();
      }
      SCHED();
#pragma unroll
      for (int i = 0; i < 5; ++i)
#pragma unroll
        for (int d = 0; d < 4; ++d) {
          gw[i][2 * d] = __builtin_amdgcn_perm(t[2 * i + 1][d], t[2 * i][d], 0x05040100u);
          gw[i][2 * d + 1] = __builtin_amdgcn_perm(t[2 * i + 1][d], t[2 * i][d], 0x07060302u);
        }
#pragma unroll
      for (int i = 0; i < 5; ++i)
#pragma unroll
        for (int c = 0; c < 8; ++c) PIN(gw[i][c]);
    }
    int wadr = L_T + (tw * 2 + ((lane >> 3) & 1)) * 64 + c8 * 8 + (lane >> 5) * 4 + ((lane >> 4) & 1) * 2;
    int radr = L_T + (lane >> 4) * 256 + (lane & 15) * 4;
    auto gemv = [&](const u32 (&hn)[5], float (&ac)[8]) {
#pragma unroll
      for (int i = 0; i < 5; ++i)
#pragma unroll
        for (int c = 0; c < 8; ++c) ac[c] = dot2(gw[i][c], hn[i], ac[c]);
      float b4[4];
#pragma unroll
      for (int c = 0; c < 4; ++c) b4[c] = tr32(ac[c], ac[c + 4]);
      *(LAS f32x2*)(L + wadr) = f32x2{tr16(b4[0], b4[2]), tr16(b4[1], b4[3])};
    };
    auto total = [&]() -> float {
      const f32x4 g = (*(const LAS f32x4*)(L + radr) + *(const LAS f32x4*)(L + radr + 64)) + (*(const LAS f32x4*)(L + radr + 128) + *(const LAS f32x4*)(L + radr + 192));
      return tr16(tr32(g[0], g[2]), tr32(g[1], g[3]));
    };
    float ac[8];
    {
      const u32 ones[5] = {0x3f803f80u, 0x3f803f80u, 0x3f803f80u, 0x3f803f80u, 0x3f803f80u};
#pragma unroll
      for (int c = 0; c < 8; ++c) ac[c] = 0.f;
      gemv(ones, ac);
    }
    WG_BARRIER();
    float cw = 0.f;
    if (tw == 0) cw = total();
    PIN(cw);
    WG_BARRIER();
#pragma unroll
    for (int c = 0; c < 8; ++c) { ac[c] = 0.f; PIN(ac[c]); }
    float e5 = c8 < 5 ? 1.0f : 0.0f;
    int sadr = WS_GU + (lane >> 4) * GU_REP + mat * 6144 + tile * 256 + (lane & 15) * 16;
    int stw = L_ST + tw, str = L_ST + c8;
    PIN(e5); PIN(sadr); PIN(wadr); PIN(radr); PIN(stw); PIN(str);
    u32x2 hv;
    {
      const int o0 = WS_HS + xcd * HS_REP + (40 * tw + 5 * r8 + (c8 < 5 ? c8 : 0)) * 8;
#pragma nounroll
      for (u32 sp = 0; sp < SPIN_CAP; ++sp) {
        CBAR();
        hv = ld_sc1_64(rs, o0);
        if (__all(hv[1] == tag)) break;
      }
    }
    u32 hn[5];
    hn[0] = __builtin_amdgcn_ds_swizzle(hv[0], 0x18 | (0 << 5));
    hn[1] = __builtin_amdgcn_ds_swizzle(hv[0], 0x18 | (1 << 5));
    hn[2] = __builtin_amdgcn_ds_swizzle(hv[0], 0x18 | (2 << 5));
    hn[3] = __builtin_amdgcn_ds_swizzle(hv[0], 0x18 | (3 << 5));
    hn[4] = __builtin_amdgcn_ds_swizzle(hv[0], 0x18 | (4 << 5));
    const float pv = lo_f(__builtin_amdgcn_readfirstlane(hv[0]));
    const float d0 = (lo_f(hv[0]) - pv) * e5, d1 = (hi_f(hv[0]) - pv) * e5;
    float s1 = sum8(d0 + d1), s2 = sum8(d0 * d0 + d1 * d1);
    s1 += dpp<ROR8>(s1); s2 += dpp<ROR8>(s2);
    float ss = tr32(s1, s2);
    ss = tr16(ss, ss);
    gemv(hn, ac);
    {
      auto sw = __builtin_amdgcn_permlane32_swap(fu(ss), fu(ss), false, false);
      const float t1 = uf(sw[0]), t2 = uf(sw[1]);
      if (lane == 0) { L[stw] = fu(pv + t1 * (1.0f / 80.0f)); L[stw + 8] = fu(t2 - t1 * t1 * (1.0f / 80.0f)); }
    }
    WG_BARRIER();
    if (tw == 0) {
      const float mw = uf(L[str]), qw = uf(L[str + 8]);
      const float G = total();
      const float mean = sum8(mw) * 0.125f;
      const float var = rbf(sum8(qw + 80.0f * (mw - mean) * (mw - mean)) * (1.0f / D));
      const float rinv = __builtin_amdgcn_rcpf(rbf(__builtin_amdgcn_sqrtf(rbf(var + 1e-5f))));
      const float g = (G - rbf(mean) * cw) * rinv;
      auto sa = __builtin_amdgcn_permlane16_swap(fu(g), fu(g), false, false);
      const u32 pr = pk_bf16(uf(sa[0]), uf(sa[1]));
      auto sb = __builtin_amdgcn_permlane32_swap(pr, pr, false, false);
      st_sc1(rs, sadr, u32x4{sb[0], tag, sb[1], tag});
    }
  }
}

extern "C" void kernel_launch(void* const* d_in, const int* in_sizes, int n_in,
                              void* d_out, int out_size, void* d_ws, size_t ws_size,
                              hipStream_t stream) {
  hipLaunchKernelGGL(fused_block, dim3(256), dim3(NTHR), 0, stream,
                     (const u16*)d_in[0], (const u16*)d_in[1], (const u16*)d_in[2], (const u16*)d_in[3], (const u16*)d_in[4],
                     (const u16*)d_in[6], (const u16*)d_in[7], (const u16*)d_in[8], (u16*)d_out, (unsigned char*)d_ws);
}
```

```cpp
#include <hip/hip_runtime.h>
#include <hip/hip_bf16.h>
#include <stdint.h>

typedef unsigned short u16;
typedef unsigned u32;
typedef unsigned long long u64;
typedef u32 u32x4 __attribute__((ext_vector_type(4)));
typedef u32 u32x2 __attribute__((ext_vector_type(2)));
typedef float f32x4 __attribute__((ext_vector_type(4)));
typedef float f32x2 __attribute__((ext_vector_type(2)));
typedef __bf16 bf16x8 __attribute__((ext_vector_type(8)));
typedef __bf16 bf16x2 __attribute__((ext_vector_type(2)));
#define GAS __attribute__((address_space(1)))
#define LAS __attribute__((address_space(3)))

constexpr int D = 640, H = 32, DH = 192, SK = 8192, W = 2048, F = 1536, HD = H * DH;
constexpr int NTHR = 768;
constexpr float LOG2E = 1.4426950408889634f;

constexpr int WIN = 8;
constexpr int WVA = 9, WVB = 7;
constexpr int KGATE = 24;
constexpr int WOWIN = 3;
constexpr int F_EARLY = 4;
constexpr int TG_QP = 355, TG_Q = 455, TG_WO = 815, TG_F = 825, TG_D = 895, TG_P = 945, TG_O = 1015, TG_R = 1155;
constexpr int LI1 = 22, LT1 = 670, LWIN = 14, TL_WO = 860;
constexpr int PF_BYTES = 15872, PF_REP = 3, PF_T0 = 100, PF_PER = 350;

constexpr int WS_EPOCH = 0;
constexpr int HS_REP = 2560;
constexpr int WS_HS = 4096;
constexpr int GU_REP = 12288;
constexpr int WS_GU = 32768;
constexpr int WS_YP = WS_GU + 4 * GU_REP;
constexpr int WS_QP = WS_YP + H * D * 4;
constexpr int OP_REC = 1024;
constexpr int WS_OP = WS_QP + H * 8 * DH * 4;
constexpr int WS_END = WS_OP + H * 8 * OP_REC;
constexpr u32 SPIN_CAP = 1u << 21;

constexpr int C_Q = 0, C_KISS = 1, C_VISS = 2, C_O = 3, C_P = 4, C_PUB = 5, C_TAG = 6, C_PF = 8;
constexpr int L_Q = 16;
constexpr int L_P = 112;
constexpr int L_ML = L_P + 384;
constexpr int L_O = L_ML + 8;
constexpr int L_T = L_O + 8 * 192;
constexpr int L_ST = L_T + 2048;
constexpr int L_X = L_ST + 16;
constexpr int L_END = L_X + 8 * 288;

__device__ __forceinline__ u32 fu(float f) { return __float_as_uint(f); }
__device__ __forceinline__ float uf(u32 u) { return __uint_as_float(u); }
__device__ __forceinline__ float lo_f(u32 u) { return uf(u << 16); }
__device__ __forceinline__ float hi_f(u32 u) { return uf(u & 0xffff0000u); }
__device__ __forceinline__ u32 pk_bf16(float lo, float hi) {
  bf16x2 v = {(__bf16)lo, (__bf16)hi};
  return __builtin_bit_cast(u32, v);
}
__device__ __forceinline__ float rbf(float f) { return (float)(__bf16)f; }
__device__ __forceinline__ float dot2(u32 a, u32 b, float acc) {
  return __builtin_amdgcn_fdot2_f32_bf16(__builtin_bit_cast(bf16x2, a), __builtin_bit_cast(bf16x2, b), acc, false);
}
__device__ __forceinline__ f32x4 mfma(u32x4 a, u32x4 b, f32x4 c) {
  return __builtin_amdgcn_mfma_f32_16x16x32_bf16(__builtin_bit_cast(bf16x8, a), __builtin_bit_cast(bf16x8, b), c, 0, 0, 0);
}
__device__ __forceinline__ u32x4 sel(u32 v, u32x4 m) { return u32x4{v & m[0], v & m[1], v & m[2], v & m[3]}; }
template <int CTRL> __device__ __forceinline__ float dpp(float x) {
  return __builtin_bit_cast(float, __builtin_amdgcn_mov_dpp(__builtin_bit_cast(int, x), CTRL, 0xf, 0xf, true));
}
template <int CTRL> __device__ __forceinline__ u32 dppu(u32 x) { return (u32)__builtin_amdgcn_mov_dpp((int)x, CTRL, 0xf, 0xf, true); }
constexpr int XOR1 = 0xB1, XOR2 = 0x4E, HMIR = 0x141, ROR8 = 0x128;
__device__ __forceinline__ float sum8(float x) { x += dpp<XOR1>(x); x += dpp<XOR2>(x); x += dpp<HMIR>(x); return x; }
__device__ __forceinline__ float max8(float x) {
  float r;
  asm volatile("s_nop 1\n\tv_max_f32_dpp %0, %1, %1 quad_perm:[1,0,3,2] row_mask:0xf bank_mask:0xf\n\t"
               "s_nop 1\n\tv_max_f32_dpp %0, %0, %0 quad_perm:[2,3,0,1] row_mask:0xf bank_mask:0xf\n\t"
               "s_nop 1\n\tv_max_f32_dpp %0, %0, %0 row_half_mirror row_mask:0xf bank_mask:0xf" : "=&v"(r) : "v"(x));
  return r;
}
__device__ __forceinline__ float max16(float x) {
  float r;
  asm volatile("s_nop 1\n\tv_max_f32_dpp %0, %1, %1 quad_perm:[1,0,3,2] row_mask:0xf bank_mask:0xf\n\t"
               "s_nop 1\n\tv_max_f32_dpp %0, %0, %0 quad_perm:[2,3,0,1] row_mask:0xf bank_mask:0xf\n\t"
               "s_nop 1\n\tv_max_f32_dpp %0, %0, %0 row_half_mirror row_mask:0xf bank_mask:0xf\n\t"
               "s_nop 1\n\tv_max_f32_dpp %0, %0, %0 row_ror:8 row_mask:0xf bank_mask:0xf\n\ts_nop 1" : "=&v"(r) : "v"(x));
  return r;
}
__device__ __forceinline__ float tr32(float a, float b) {
  auto t = __builtin_amdgcn_permlane32_swap(fu(a), fu(b), false, false);
  return uf(t[0]) + uf(t[1]);
}
__device__ __forceinline__ float tr16(float a, float b) {
  auto t = __builtin_amdgcn_permlane16_swap(fu(a), fu(b), false, false);
  return uf(t[0]) + uf(t[1]);
}
__device__ __forceinline__ float xrow_sum(float x) { x = tr16(x, x); return tr32(x, x); }
__device__ __forceinline__ float xrow_max(float x) {
  auto s = __builtin_amdgcn_permlane16_swap(fu(x), fu(x), false, false);
  x = fmaxf(uf(s[0]), uf(s[1]));
  auto t = __builtin_amdgcn_permlane32_swap(fu(x), fu(x), false, false);
  return fmaxf(uf(t[0]), uf(t[1]));
}
__device__ __forceinline__ float sum64(float x) { x = sum8(x); x += dpp<ROR8>(x); return xrow_sum(x); }
__device__ __forceinline__ f32x4 sum_r8(f32x4 t) {
  float w = tr16(tr32(t[0], t[1]), tr32(t[2], t[3]));
  w += dpp<ROR8>(w);
  auto sa = __builtin_amdgcn_permlane16_swap(fu(w), fu(w), false, false);
  auto se = __builtin_amdgcn_permlane32_swap(sa[0], sa[0], false, false);
  auto so = __builtin_amdgcn_permlane32_swap(sa[1], sa[1], false, false);
  return f32x4{uf(se[0]), uf(se[1]), uf(so[0]), uf(so[1])};
}

typedef __amdgpu_buffer_rsrc_t rsrc_t;
__device__ __forceinline__ rsrc_t mkrs(const void* p, int bytes) { return __builtin_amdgcn_make_buffer_rsrc((void*)p, 0, bytes, 0x00020000); }
__device__ __forceinline__ u32x4 ld_nt(rsrc_t rs, int off) { return __builtin_amdgcn_raw_buffer_load_b128(rs, off, 0, 2); }
__device__ __forceinline__ u32x4 ld_sc1(rsrc_t rs, int off) { return __builtin_amdgcn_raw_buffer_load_b128(rs, off, 0, 16); }
__device__ __forceinline__ u32x2 ld_sc1_64(rsrc_t rs, int off) { return __builtin_amdgcn_raw_buffer_load_b64(rs, off, 0, 16); }
__device__ __forceinline__ void st_sc1(rsrc_t rs, int off, u32x4 v) { __builtin_amdgcn_raw_buffer_store_b128(v, rs, off, 0, 16); }
template <int N0, int N1, int N2> __device__ __forceinline__ void vmw3(int mode) {
  asm volatile("s_cmp_eq_u32 %0, 2\n\ts_cbranch_scc1 1f\n\ts_waitcnt vmcnt(%2)\n\ts_cmp_eq_u32 %0, 1\n\ts_cbranch_scc1 1f\n\t"
               "s_waitcnt vmcnt(%1)\n1:\n\ts_waitcnt vmcnt(%3)" :: "s"(mode), "i"(N0), "i"(N1), "i"(N2) : "memory", "scc");
}
template <int N> __device__ __forceinline__ void vmw() { asm volatile("s_waitcnt vmcnt(%0)" :: "i"(N) : "memory"); }

__device__ __forceinline__ u32x4 pack4(f32x4 v, u32 tag) {
  const u32 a = fu(v[0]) + 0x80u, b = fu(v[1]) + 0x80u, c = fu(v[2]) + 0x80u, d = fu(v[3]) + 0x80u;
  return u32x4{__builtin_amdgcn_perm(a, b, 0x07060503u), __builtin_amdgcn_perm(b, tag, 0x06050100u),
               __builtin_amdgcn_perm(c, d, 0x07060503u), __builtin_amdgcn_perm(d, tag, 0x06050100u)};
}
__device__ __forceinline__ f32x4 unpack4(u32x4 w) {
  return f32x4{uf(w[0] & 0xffffff00u), uf(__builtin_amdgcn_perm(w[0], w[1], 0x0403020cu)),
               uf(w[2] & 0xffffff00u), uf(__builtin_amdgcn_perm(w[2], w[3], 0x0403020cu))};
}
__device__ __forceinline__ u32x4 packq(f32x4 v, u32 tag) {
  const u32 e = max(max(fu(v[0]) & 0x7fffffffu, fu(v[1]) & 0x7fffffffu), max(fu(v[2]) & 0x7fffffffu, fu(v[3]) & 0x7fffffffu)) >> 23;
  u32 m[4];
#pragma unroll
  for (int i = 0; i < 4; ++i) m[i] = (u32)min(max((int)__builtin_rintf(__builtin_amdgcn_ldexpf(v[i], 147 - (int)e)), -2097151), 2097151);
  const u32 t = tag & 0xffffu;
  return u32x4{(m[0] << 10) | ((m[1] >> 12) & 0x3ffu), (m[1] << 20) | ((e & 15u) << 16) | t,
               (m[2] << 10) | ((m[3] >> 12) & 0x3ffu), (m[3] << 20) | ((e >> 4) << 16) | t};
}
__device__ __forceinline__ f32x4 unpackq(u32x4 w) {
  const int e = (int)(((w[1] >> 16) & 15u) | ((w[3] >> 12) & 0xf0u));
  const int m[4] = {(int)w[0] >> 10, (int)__builtin_amdgcn_alignbit(w[0], w[1], 10) >> 10, (int)w[2] >> 10, (int)__builtin_amdgcn_alignbit(w[2], w[3], 10) >> 10};
  f32x4 r;
#pragma unroll
  for (int i = 0; i < 4; ++i) r[i] = e == 255 ? __builtin_nanf("") : __builtin_amdgcn_ldexpf((float)m[i], e - 147);
  return r;
}
__device__ __forceinline__ bool ok4(u32x4 w, u32 tag) { return (((w[1] ^ tag) | (w[3] ^ tag)) & 0xffffu) == 0u; }

#define CBAR() asm volatile("" ::: "memory")
#define PIN(x) asm volatile("" : "+v"(x))
#define LGKM0() asm volatile("s_waitcnt lgkmcnt(0)" ::: "memory")
#define WG_BARRIER() asm volatile("s_waitcnt lgkmcnt(0)\n\ts_barrier" ::: "memory")
#define SCHED() __builtin_amdgcn_sched_barrier(0)

__device__ __forceinline__ void lds_wait_ge(volatile LAS u32* w, u32 n) {
#pragma nounroll
  for (u32 sp = 0; sp < SPIN_CAP; ++sp) {
    if (*w >= n) break;
    __builtin_amdgcn_s_sleep(1);
  }
  CBAR();
}
__device__ __forceinline__ void wait_until(u64 t) {
#pragma nounroll
  for (u32 sp = 0; sp < 4096; ++sp) {
    if (__builtin_amdgcn_s_memrealtime() >= t) break;
    __builtin_amdgcn_s_sleep(1);
  }
  CBAR();
}
__device__ __forceinline__ void lds_inc(LAS u32* w, int lane) {
  CBAR();
  if (lane == 0) __hip_atomic_fetch_add(w, 1u, __ATOMIC_RELAXED, __HIP_MEMORY_SCOPE_WORKGROUP);
  CBAR();
}

__global__ void __launch_bounds__(NTHR, 3)
fused_block(const u16* __restrict__ x_, const u16* __restrict__ kc_, const u16* __restrict__ vc_, const u16* __restrict__ wq_,
            const u16* __restrict__ wo_, const u16* __restrict__ wg_, const u16* __restrict__ wu_, const u16* __restrict__ wd_,
            u16* __restrict__ out_, unsigned char* ws_) {
  u64 pc0; asm volatile("s_getpc_b64 %0" : "=s"(pc0));
  SCHED();
  __shared__ __attribute__((aligned(16))) u32 smem[L_END];
  LAS u32* L = (LAS u32*)smem;
  volatile LAS u32* LV = (volatile LAS u32*)smem;
  const int tid = threadIdx.x, lane = tid & 63, wave = __builtin_amdgcn_readfirstlane(tid >> 6);
  const int b = blockIdx.x;
  const int xcd = b & 7, idx = b >> 3;
  const int h = xcd * 4 + (idx >> 3), s = idx & 7;
  rsrc_t rs;
  const int r8 = lane >> 3, c8 = lane & 7;
  u64 tstart;
  u32x4 msk;
#pragma unroll
  for (int d = 0; d < 4; ++d) msk[d] = (d == (c8 >> 1)) ? ((c8 & 1) ? 0xffff0000u : 0x0000ffffu) : 0u;
  u32 tag;
#define START() do { unsigned char* w_ = ws_; asm volatile("" : "+s"(w_) : "s"(x_), "s"(kc_), "s"(vc_), "s"(wq_), "s"(wo_), "s"(wg_), "s"(wu_), "s"(wd_), "s"(out_)); \
    rs = mkrs(w_, WS_END); tstart = __builtin_amdgcn_s_memrealtime(); SCHED(); } while (0)
#define FIRST_BARRIER() do { WG_BARRIER(); tag = __builtin_amdgcn_readfirstlane(LV[C_TAG]); } while (0)
  const int g0 = 36 * s + (s > 3 ? s - 3 : 0), ng = s >= 3 ? 37 : 36;
  const size_t kvoff = ((size_t)h * SK + (SK - W) + g0 * 8) * DH;

  if (wave < 8 && s == 7) {
    START();
    FIRST_BARRIER();
    if (wave == 2 && idx >= 16) {
      const rsrc_t rc = mkrs((const void*)(pc0 & ~127ull), PF_BYTES);
      const int off = ((3 - (idx >> 3)) * 64 + lane) * 128;
#pragma nounroll
      for (int r = 0; r < PF_REP; ++r) {
        wait_until(tstart + PF_T0 + r * PF_PER);
        u32 t1 = __builtin_amdgcn_raw_buffer_load_b32(rc, off, 0, 2);
        asm volatile("" :: "v"(t1));
      }
    }
  } else if (wave < 4) {
    __builtin_amdgcn_s_setprio(2);
    int loff = wave * 3072 + r8 * 384 + c8 * 16;
    PIN(loff);
    START();
    const rsrc_t rk = mkrs(kc_ + kvoff, ng * 3072);
    u32x4 kr[30];
#pragma unroll
    for (int i = 0; i < 30; ++i) {
      if (i == WIN) FIRST_BARRIER();
      if (i >= WIN) vmw<WIN - 1>();
      if (i == KGATE) lds_inc(L + C_KISS, lane);
      kr[i] = ld_nt(rk, loff + (i / 3) * 12288 + (i % 3) * 128);
      SCHED();
    }
    float dd[10];
    const bool sbok = c8 == 0 || (c8 == 1 && 36 + wave < ng);
    int padr = L_P + wave * 96 + r8 * 12 + c8, padr2 = c8 < 2 ? padr + 8 : L_T + lane;
    PIN(padr); PIN(padr2);
#pragma unroll
    for (int g = 0; g < 10; ++g) { dd[g] = 0.f; PIN(dd[g]); }
    lds_wait_ge(LV + C_Q, 3);
    u32x4 qp[3];
#pragma unroll
    for (int t = 0; t < 3; ++t) qp[t] = *(const LAS u32x4*)(L + L_Q + t * 32 + c8 * 4);
#pragma unroll
    for (int e = 0; e < 12; ++e) {
#pragma unroll
      for (int g = 0; g < 10; ++g) dd[g] = dot2(kr[g * 3 + e / 4][e % 4], qp[e / 4][e % 4], dd[g]);
      SCHED();
    }
    const bool b0 = c8 & 1, b1 = c8 & 2, b2 = c8 & 4;
    float e4[4], e2[2];
#pragma unroll
    for (int i = 0; i < 4; ++i) { const float keep = b2 ? dd[i + 4] : dd[i], give = b2 ? dd[i] : dd[i + 4]; e4[i] = keep + dpp<HMIR>(give); }
#pragma unroll
    for (int i = 0; i < 2; ++i) { const float keep = b1 ? e4[i + 2] : e4[i], give = b1 ? e4[i] : e4[i + 2]; e2[i] = keep + dpp<XOR2>(give); }
    float sa = (b0 ? e2[1] : e2[0]) + dpp<XOR1>(b0 ? e2[0] : e2[1]);
    float sb = (b0 ? dd[9] : dd[8]) + dpp<XOR1>(b0 ? dd[8] : dd[9]);
    sb += dpp<XOR2>(sb);
    sb += dpp<0x1B>(dpp<HMIR>(sb));
    sa = rbf(rbf(sa) * 0.07216878364870323f);
    sb = rbf(rbf(sb) * 0.07216878364870323f);
    sb = sbok ? sb : -INFINITY;
    const float m = xrow_max(max16(fmaxf(sa, sb)));
    const float pa = __builtin_amdgcn_exp2f((sa - m) * LOG2E), pb = __builtin_amdgcn_exp2f((sb - m) * LOG2E);
    L[padr] = pk_bf16(pa, pa);
    L[padr2] = pk_bf16(pb, pb);
    CBAR();
    if (lane == 0) LV[C_PF + wave] = 1u;
    const float l = sum64(pa + pb);
    if (lane == 0) { L[L_ML + wave] = fu(m); L[L_ML + 4 + wave] = fu(l); }
    lds_inc(L + C_P, lane);
  } else if (wave < 8) {
    __builtin_amdgcn_s_setprio(1);
    START();
    const int w = wave - 4;
    const rsrc_t rv = mkrs(vc_ + kvoff, ng * 3072);
    const int loff = w * 3072 + r8 * 384 + c8 * 16;
    FIRST_BARRIER();
    lds_wait_ge(LV + C_KISS, 4);
    u32x4 vr[30];
    int mode = __builtin_amdgcn_readfirstlane(s < 5 ? 1 : 0);
#pragma unroll
    for (int i = 0; i < 30; ++i) {
      const int j = (i + 27) % 30;
      if (i == LI1) mode = __builtin_amdgcn_readfirstlane(__builtin_amdgcn_s_memrealtime() > tstart + LT1 ? 2 : mode);
      if (i >= WVB) vmw3<WVB - 1, WVA - 1, LWIN - 1>(mode);
      if (i == 29) lds_inc(L + C_VISS, lane);
      vr[j] = ld_nt(rv, loff + (j / 3) * 12288 + (j % 3) * 128);
      SCHED();
    }
    int wadr = ((lane >> 5) == ((lane >> 3) & 1) ? L_O + (w * 2 + (lane >> 5)) * 192 : L_T + 64 + w * 192) + c8 * 8 + ((lane >> 4) & 1) * 4;
    int radr = L_O + 24 * r8 + 4 * (c8 < 6 ? c8 : 0);
    int sadr = WS_OP + (h * 8 + s) * OP_REC + lane * 16;
    u32 tadr = (u32)(uintptr_t)(L + C_O), one = 1u;
    PIN(wadr); PIN(radr); PIN(sadr); PIN(tadr); PIN(one);
    lds_wait_ge(LV + C_PF + w, 1);
    const LAS u32* pr = L + L_P + w * 96 + r8 * 12;
    const u32x4 p0 = *(const LAS u32x4*)(pr), p1 = *(const LAS u32x4*)(pr + 4);
    const u32x2 p2 = *(const LAS u32x2*)(pr + 8);
    float M, Ls, fw;
    f32x4 acc[2][3];
#pragma unroll
    for (int gg = 0; gg < 10; ++gg) {
      const int g = (gg + 9) % 10;
      if (gg == 5) {
        SCHED();
        lds_wait_ge(LV + C_P, 4);
        const f32x4 mm = *(const LAS f32x4*)(L + L_ML), ll = *(const LAS f32x4*)(L + L_ML + 4);
        M = fmaxf(fmaxf(mm[0], mm[1]), fmaxf(mm[2], mm[3]));
        f32x4 ff;
#pragma unroll
        for (int k = 0; k < 4; ++k) ff[k] = __builtin_amdgcn_exp2f((mm[k] - M) * LOG2E);
        Ls = (ff[0] * ll[0] + ff[1] * ll[1]) + (ff[2] * ll[2] + ff[3] * ll[3]);
        fw = w == 0 ? ff[0] : (w == 1 ? ff[1] : (w == 2 ? ff[2] : ff[3]));
        PIN(M); PIN(Ls); PIN(fw);
      }
      const u32x4 A = sel(g < 4 ? p0[g & 3] : (g < 8 ? p1[g & 3] : p2[g & 1]), msk);
#pragma unroll
      for (int t = 0; t < 3; ++t) {
        if (gg == 9) acc[0][t] = mfma(A, vr[g * 3 + t], acc[0][t] + acc[1][t]);
        else acc[gg & 1][t] = mfma(A, vr[g * 3 + t], gg < 2 ? f32x4{0.f, 0.f, 0.f, 0.f} : acc[gg & 1][t]);
      }
    }
#pragma unroll
    for (int t = 0; t < 3; ++t) *(LAS f32x4*)(L + wadr + t * 64) = acc[0][t] * fw;
    u32 old;
    asm volatile("s_mov_b64 exec, 1\n\tds_add_rtn_u32 %0, %1, %2\n\ts_mov_b64 exec, -1\n\ts_waitcnt lgkmcnt(0)" : "=&v"(old) : "v"(tadr), "v"(one) : "memory");
    if (__builtin_amdgcn_readfirstlane(old) == 3) {
      const u64 tnow = __builtin_amdgcn_s_memrealtime();
      f32x4 pt[8];
#pragma unroll
      for (int k = 0; k < 8; ++k) pt[k] = *(const LAS f32x4*)(L + radr + k * 192);
      SCHED();
      f32x4 o = ((pt[0] + pt[1]) + (pt[2] + pt[3])) + ((pt[4] + pt[5]) + (pt[6] + pt[7]));
      if (c8 == 6) { o[0] = M; o[1] = Ls; }
      u32x4 pk = pack4(o, tag);
      PIN(pk);
      if (tnow < tstart + TG_P) wait_until(tstart + TG_P);
      st_sc1(rs, sadr, pk);
      if (lane == 0) LV[C_PUB] = 1u;
    }
    return;
  } else {
    const int a = wave - 8;
    __builtin_amdgcn_s_setprio(3);
    if (a < 3) {
      int qoff = (80 * s + 10 * r8) * (HD * 2) + h * (DH * 2) + a * 128 + c8 * 16, xo1 = lane * 16, xo2 = 1024 + lane * 4, xo3 = 160 * s + 20 * r8;
      PIN(qoff); PIN(xo1); PIN(xo2); PIN(xo3);
      START();
      const rsrc_t rx = mkrs(x_, D * 2), rq = mkrs(wq_, D * HD * 2);
      const u32x4 xa = __builtin_amdgcn_raw_buffer_load_b128(rx, xo1, 0, 0);
      const u32 xc = __builtin_amdgcn_raw_buffer_load_b32(rx, xo2, 0, 0);
      const u32x4 xq = __builtin_amdgcn_raw_buffer_load_b128(rx, xo3, 0, 0);
      const u32 xo[5] = {xq[0], xq[1], xq[2], xq[3], __builtin_amdgcn_raw_buffer_load_b32(rx, xo3 + 16, 0, 0)};
      u32x4 wr[10];
#pragma unroll
      for (int g = 0; g < 10; ++g) wr[g] = ld_nt(rq, qoff + g * (HD * 2));
      SCHED();
      FIRST_BARRIER();
      SCHED();
      const float xv[10] = {lo_f(xa[0]), hi_f(xa[0]), lo_f(xa[1]), hi_f(xa[1]), lo_f(xa[2]), hi_f(xa[2]), lo_f(xa[3]), hi_f(xa[3]), lo_f(xc), hi_f(xc)};
      float sm = 0.f;
#pragma unroll
      for (int i = 0; i < 10; ++i) sm += xv[i];
      const float mean = sum64(sm) * (1.0f / D);
      float sq = 0.f;
#pragma unroll
      for (int i = 0; i < 10; ++i) { const float dd = xv[i] - mean; sq += dd * dd; }
      const float var = rbf(sum64(sq) * (1.0f / D));
      const float mb = rbf(mean);
      const float rinv = 1.0f / rbf(__builtin_sqrtf(rbf(var + 1e-5f)));
      f32x4 acc = {0.f, 0.f, 0.f, 0.f};
#pragma unroll
      for (int g = 0; g < 10; ++g) {
        const float xg = (g & 1) ? hi_f(xo[g >> 1]) : lo_f(xo[g >> 1]);
        const float xn = rbf(xg - mb) * rinv;
        acc = mfma(sel(pk_bf16(xn, xn), msk), wr[g], acc);
      }
#pragma unroll
      for (int i = 0; i < 4; ++i) {
        const float up = dpp<ROR8>(acc[i]);
        auto t = __builtin_amdgcn_permlane32_swap(fu(up), fu(up), false, false);
        acc[i] += uf(t[1]);
      }
      u32x4 pk = packq(acc, tag);
      PIN(pk);
      wait_until(tstart + TG_QP);
      if (lane < 32 && (lane & 8) == 0) st_sc1(rs, WS_QP + (h * 8 + s) * (DH * 4) + a * 256 + (c8 + 8 * (lane >> 4)) * 16, pk);
      if (s < 7) {
        const int gb = WS_QP + (h * 8 + (lane >> 4)) * (DH * 4) + a * 256 + (lane & 15) * 16;
        u32x4 v[2];
        bool ok[2] = {false, false};
        wait_until(tstart + TG_Q);
#pragma nounroll
        for (u32 sp = 0; sp < SPIN_CAP; ++sp) {
          CBAR();
#pragma unroll
          for (int i = 0; i < 2; ++i) if (!ok[i]) v[i] = ld_sc1(rs, gb + i * (4 * DH * 4));
#pragma unroll
          for (int i = 0; i < 2; ++i) ok[i] = ok4(v[i], tag);
          if (__all(ok[0] & ok[1])) break;
          __builtin_amdgcn_s_sleep(2);
        }
        f32x4 q = unpackq(v[0]) + unpackq(v[1]);
#pragma unroll
        for (int i = 0; i < 4; ++i) q[i] = xrow_sum(q[i]);
        if (lane < 16) *(LAS u32x2*)(L + L_Q + a * 32 + 4 * c8 + 2 * (lane >> 3)) = u32x2{pk_bf16(q[0], q[1]), pk_bf16(q[2], q[3])};
        lds_inc(L + C_Q, lane);
      }
    } else {
      START();
      u32 epw;
      asm volatile("s_load_dword %0, %1, 0x0\n\ts_waitcnt lgkmcnt(0)" : "=&s"(epw) : "s"(ws_) : "memory");
      if (lane < 16) L[lane] = lane == C_TAG ? epw + 1u : 0u;
      FIRST_BARRIER();
    }
  }
  __builtin_amdgcn_s_setprio(0);

  if (s == 7) {
    const int j4 = idx >> 3;
    const int rI = j4 < 2 ? 2 * xcd + j4 : 14 + 2 * xcd + j4;
    if (rI < 20) {
      const int dt = rI;
      const int r16 = lane >> 2, c4 = lane & 3;
      u32 dw[4][8];
      {
        const rsrc_t rd = mkrs(wd_ + (size_t)128 * wave * D + dt * 32, 127 * D * 2 + 64);
        const int doff = 8 * r16 * (D * 2) + c4 * 16;
        wait_until(tstart + TG_D);
        u32x4 t[8];
#pragma unroll
        for (int i = 0; i < 8; ++i) { t[i] = ld_nt(rd, doff + i * (D * 2)); SCHED(); }
#pragma unroll
        for (int k = 0; k < 4; ++k)
#pragma unroll
          for (int d = 0; d < 4; ++d) {
            dw[k][2 * d] = __builtin_amdgcn_perm(t[2 * k + 1][d], t[2 * k][d], 0x05040100u);
            dw[k][2 * d + 1] = __builtin_amdgcn_perm(t[2 * k + 1][d], t[2 * k][d], 0x07060302u);
          }
#pragma unroll
        for (int k = 0; k < 4; ++k)
#pragma unroll
          for (int c = 0; c < 8; ++c) PIN(dw[k][c]);
      }
      float ac[8];
#pragma unroll
      for (int c = 0; c < 8; ++c) { ac[c] = 0.f; PIN(ac[c]); }
      int wadr = L_T + (wave * 4 + ((lane >> 2) & 3)) * 32 + c4 * 8 + (lane >> 5) * 4 + ((lane >> 4) & 1) * 2;
      int radr = L_T + r8 * 192 + c8 * 4;
      int oadr = dt * 64 + c8 * 8;
      PIN(wadr); PIN(radr); PIN(oadr);
      const rsrc_t ro = mkrs(out_, D * 2);
      u32x2 gv, uv;
      u32x4 hv = {0u, tag, 0u, tag};
      {
        const int o0 = WS_GU + ((rI >> 1) & 3) * GU_REP + (64 * wave + lane) * 8;
        const int oh = WS_HS + xcd * HS_REP + (dt * 16 + c8 * 2) * 8;
#pragma nounroll
        for (u32 sp = 0; sp < SPIN_CAP; ++sp) {
          CBAR();
          gv = ld_sc1_64(rs, o0); uv = ld_sc1_64(rs, o0 + 6144);
          if (wave == 0) hv = ld_sc1(rs, oh);
          if (__all(((gv[1] ^ tag) | (uv[1] ^ tag) | (hv[1] ^ tag) | (hv[3] ^ tag)) == 0u)) break;
        }
      }
      auto glu = [](float g, float u) -> float { return rbf(__builtin_amdgcn_rcpf(1.0f + __builtin_amdgcn_exp2f(-g * LOG2E))) * u; };
      const u32 ha = pk_bf16(glu(lo_f(gv[0]), lo_f(uv[0])), glu(hi_f(gv[0]), hi_f(uv[0])));
      const u32 hh[4] = {dppu<0x00>(ha), dppu<0x55>(ha), dppu<0xAA>(ha), dppu<0xFF>(ha)};
#pragma unroll
      for (int k = 0; k < 4; ++k)
#pragma unroll
        for (int c = 0; c < 8; ++c) ac[c] = dot2(dw[k][c], hh[k], ac[c]);
      float b4[4];
#pragma unroll
      for (int c = 0; c < 4; ++c) b4[c] = tr32(ac[c], ac[c + 4]);
      *(LAS f32x2*)(L + wadr) = f32x2{tr16(b4[0], b4[2]), tr16(b4[1], b4[3])};
      WG_BARRIER();
      if (wave == 0) {
        f32x4 p[6];
#pragma unroll
        for (int k = 0; k < 6; ++k) p[k] = *(const LAS f32x4*)(L + radr + k * 32);
        const f32x4 t = sum_r8(((p[0] + p[1]) + (p[2] + p[3])) + (p[4] + p[5]));
        const u32 d0 = pk_bf16(t[0], t[1]), d1 = pk_bf16(t[2], t[3]);
        const u32x2 ov = {pk_bf16(lo_f(hv[0]) + lo_f(d0), hi_f(hv[0]) + hi_f(d0)), pk_bf16(lo_f(hv[2]) + lo_f(d1), hi_f(hv[2]) + hi_f(d1))};
        if (lane < 8) __builtin_amdgcn_raw_buffer_store_b64(ov, ro, oadr, 0, 16);
        if (rI == 0 && lane == 0) __builtin_amdgcn_raw_buffer_store_b32(tag, rs, WS_EPOCH, 0, 16);
      }
    } else if (rI < 30 && wave < 2) {
      const int ru = (rI - 20) * 2 + wave;
      const u32x2 xp = *(const GAS u32x2*)((const GAS char*)x_ + (ru * 32 + c8 * 4) * 2);
      f32x4 xf = {lo_f(xp[0]), hi_f(xp[0]), lo_f(xp[1]), hi_f(xp[1])};
      int sadr = WS_HS + r8 * HS_REP + (ru * 16 + c8 * 2) * 8;
      PIN(xf); PIN(sadr);
      const int gb = WS_YP + (r8 * D + ru * 32 + c8 * 4) * 4;
      u32x4 v[4];
      bool ok[4] = {false, false, false, false};
      wait_until(tstart + TG_R);
#pragma nounroll
      for (u32 sp = 0; sp < SPIN_CAP; ++sp) {
        CBAR();
#pragma unroll
        for (int i = 0; i < 4; ++i) if (!ok[i]) v[i] = ld_sc1(rs, gb + i * (8 * D * 4));
#pragma unroll
        for (int i = 0; i < 4; ++i) ok[i] = ok4(v[i], tag);
        if (__all(ok[0] & ok[1] & ok[2] & ok[3])) break;
      }
      const f32x4 y = sum_r8((unpack4(v[0]) + unpack4(v[1])) + (unpack4(v[2]) + unpack4(v[3])));
      const u32 a0 = pk_bf16(y[0], y[1]), a1 = pk_bf16(y[2], y[3]);
      st_sc1(rs, sadr, u32x4{pk_bf16(xf[0] + lo_f(a0), xf[1] + hi_f(a0)), tag, pk_bf16(xf[2] + lo_f(a1), xf[3] + hi_f(a1)), tag});
    }
    return;
  }

  const int tw = wave < 4 ? wave : wave - 4;
  if (s < 5) {
    lds_wait_ge(LV + C_VISS, 4);
    const int g4 = lane >> 4, z = lane & 15;
    const rsrc_t ro = mkrs(wo_ + (size_t)(h * DH + 24 * tw) * D + s * 128, 23 * D * 2 + 256);
    const int ooff = 6 * g4 * (D * 2) + z * 16;
    u32x4 wr[6];
    wait_until(tstart + TG_WO);
    const bool paced = __builtin_amdgcn_s_memrealtime() < tstart + TL_WO;
#pragma unroll
    for (int k = 0; k < 6; ++k) { if (k >= WOWIN && paced) vmw<WOWIN - 1>(); wr[k] = ld_nt(ro, ooff + k * (D * 2)); SCHED(); }
    u32x4 mA, mB;
#pragma unroll
    for (int d = 0; d < 4; ++d) { mA[d] = z < 8 ? msk[d] : 0u; mB[d] = z < 8 ? 0u : msk[d]; }
    int xw = L_X + tw * 288 + r8 * 36 + c8 * 4, xm = L_X + tw * 288 + c8 * 36 + 24, xo = L_X + tw * 288 + c8 * 36 + 3 * r8;
    int wadr = L_T + (tw * 2 + (g4 >> 1)) * 128 + z * 8 + (g4 & 1) * 4;
    int radr = L_T + r8 * 256 + tw * 32 + c8 * 4;
    int sadr = WS_YP + (h * D + s * 128 + tw * 32 + c8 * 4) * 4;
    PIN(mA); PIN(mB); PIN(xw); PIN(xm); PIN(xo); PIN(wadr); PIN(radr); PIN(sadr);
    lds_wait_ge(LV + C_PUB, 1);
    const int gb = WS_OP + (h * 8 + r8) * OP_REC + tw * 128 + c8 * 16;
    u32x4 va = {c8 == 6 ? 0xff800000u : 0u, tag & 0xffffu, 0u, tag & 0xffffu};
    bool ok = r8 == 7;
    wait_until(tstart + TG_O);
#pragma nounroll
    for (u32 sp = 0; sp < SPIN_CAP; ++sp) {
      CBAR();
      if (!ok) va = ld_sc1(rs, gb);
      ok = ok4(va, tag);
      if (__all(ok)) break;
    }
    *(LAS f32x4*)(L + xw) = unpack4(va);
    CBAR();
    const f32x2 ml = *(const LAS f32x2*)(L + xm);
    const float o0 = uf(L[xo]), o1 = uf(L[xo + 1]), o2 = uf(L[xo + 2]);
    SCHED();
    const float f = __builtin_amdgcn_exp2f((ml[0] - max8(ml[0])) * LOG2E);
    float t0 = f * o0, t1 = f * o1, t2 = f * o2, t3 = f * ml[1];
    t0 += dpp<XOR1>(t0); t1 += dpp<XOR1>(t1); t2 += dpp<XOR1>(t2); t3 += dpp<XOR1>(t3);
    t3 += dpp<XOR2>(t3); t0 += dpp<XOR2>(t0); t1 += dpp<XOR2>(t1); t2 += dpp<XOR2>(t2);
    t3 += dpp<HMIR>(t3); t0 += dpp<HMIR>(t0); t1 += dpp<HMIR>(t1); t2 += dpp<HMIR>(t2);
    const float li = __builtin_amdgcn_rcpf(t3);
    t0 *= li; t1 *= li; t2 *= li;
    const u32 v0 = pk_bf16(t0, t0), v1 = pk_bf16(t1, t1), v2 = pk_bf16(t2, t2);
    const f32x4 zero = {0.f, 0.f, 0.f, 0.f};
    f32x4 aA = mfma(sel(v0, mA), wr[0], zero), aB = mfma(sel(v0, mB), wr[3], zero);
    aA = mfma(sel(v1, mA), wr[1], aA); aB = mfma(sel(v1, mB), wr[4], aB);
    aA = mfma(sel(v2, mA), wr[2], aA);
    aA = mfma(sel(v2, mB), wr[5], aA);
    *(LAS f32x4*)(L + wadr) = aA + aB;
    WG_BARRIER();
    if (tw < 4) {
      const f32x4 t = sum_r8(*(const LAS f32x4*)(L + radr) + *(const LAS f32x4*)(L + radr + 128));
      const u32x4 pk = pack4(t, tag);
      if (lane < 8) st_sc1(rs, sadr, pk);
    }
    return;
  }

  const int fi = (idx >> 3) * 2 + (s - 5);
  if (fi < 6) {
    const int ft = xcd * 6 + fi, mat = ft & 1, tile = ft >> 1;
    lds_wait_ge(LV + C_VISS, 4);
    u32 gw[5][8];
    {
      const rsrc_t rg = mkrs((mat ? wu_ : wg_) + (size_t)80 * tw * F + tile * 64, 79 * F * 2 + 128);
      const int goff = 10 * r8 * (F * 2) + c8 * 16;
      u32x4 t[10];
      wait_until(tstart + TG_F);
#pragma unroll
      for (int i = 0; i < 10; ++i) {
        if (i == F_EARLY) { SCHED(); lds_wait_ge(LV + C_PUB, 1); SCHED(); }
        t[i] = ld_nt(rg, goff + i * (F * 2));
        SCHED();
      }
      SCHED();
#pragma unroll
      for (int i = 0; i < 5; ++i)
#pragma unroll
        for (int d = 0; d < 4; ++d) {
          gw[i][2 * d] = __builtin_amdgcn_perm(t[2 * i + 1][d], t[2 * i][d], 0x05040100u);
          gw[i][2 * d + 1] = __builtin_amdgcn_perm(t[2 * i + 1][d], t[2 * i][d], 0x07060302u);
        }
#pragma unroll
      for (int i = 0; i < 5; ++i)
#pragma unroll
        for (int c = 0; c < 8; ++c) PIN(gw[i][c]);
    }
    int wadr = L_T + (tw * 2 + ((lane >> 3) & 1)) * 64 + c8 * 8 + (lane >> 5) * 4 + ((lane >> 4) & 1) * 2;
    int radr = L_T + (lane >> 4) * 256 + (lane & 15) * 4;
    auto gemv = [&](const u32 (&hn)[5], float (&ac)[8]) {
#pragma unroll
      for (int i = 0; i < 5; ++i)
#pragma unroll
        for (int c = 0; c < 8; ++c) ac[c] = dot2(gw[i][c], hn[i], ac[c]);
      float b4[4];
#pragma unroll
      for (int c = 0; c < 4; ++c) b4[c] = tr32(ac[c], ac[c + 4]);
      *(LAS f32x2*)(L + wadr) = f32x2{tr16(b4[0], b4[2]), tr16(b4[1], b4[3])};
    };
    auto total = [&]() -> float {
      const f32x4 g = (*(const LAS f32x4*)(L + radr) + *(const LAS f32x4*)(L + radr + 64)) + (*(const LAS f32x4*)(L + radr + 128) + *(const LAS f32x4*)(L + radr + 192));
      return tr16(tr32(g[0], g[2]), tr32(g[1], g[3]));
    };
    float ac[8];
    {
      const u32 ones[5] = {0x3f803f80u, 0x3f803f80u, 0x3f803f80u, 0x3f803f80u, 0x3f803f80u};
#pragma unroll
      for (int c = 0; c < 8; ++c) ac[c] = 0.f;
      gemv(ones, ac);
    }
    WG_BARRIER();
    float cw = 0.f;
    if (tw == 0) cw = total();
    PIN(cw);
    WG_BARRIER();
#pragma unroll
    for (int c = 0; c < 8; ++c) { ac[c] = 0.f; PIN(ac[c]); }
    float e5 = c8 < 5 ? 1.0f : 0.0f;
    int sadr = WS_GU + (lane >> 4) * GU_REP + mat * 6144 + tile * 256 + (lane & 15) * 16;
    int stw = L_ST + tw, str = L_ST + c8;
    PIN(e5); PIN(sadr); PIN(wadr); PIN(radr); PIN(stw); PIN(str);
    u32x2 hv;
    {
      const int o0 = WS_HS + xcd * HS_REP + (40 * tw + 5 * r8 + (c8 < 5 ? c8 : 0)) * 8;
#pragma nounroll
      for (u32 sp = 0; sp < SPIN_CAP; ++sp) {
        CBAR();
        hv = ld_sc1_64(rs, o0);
        if (__all(hv[1] == tag)) break;
      }
    }
    u32 hn[5];
    hn[0] = __builtin_amdgcn_ds_swizzle(hv[0], 0x18 | (0 << 5));
    hn[1] = __builtin_amdgcn_ds_swizzle(hv[0], 0x18 | (1 << 5));
    hn[2] = __builtin_amdgcn_ds_swizzle(hv[0], 0x18 | (2 << 5));
    hn[3] = __builtin_amdgcn_ds_swizzle(hv[0], 0x18 | (3 << 5));
    hn[4] = __builtin_amdgcn_ds_swizzle(hv[0], 0x18 | (4 << 5));
    const float pv = lo_f(__builtin_amdgcn_readfirstlane(hv[0]));
    const float d0 = (lo_f(hv[0]) - pv) * e5, d1 = (hi_f(hv[0]) - pv) * e5;
    float s1 = sum8(d0 + d1), s2 = sum8(d0 * d0 + d1 * d1);
    s1 += dpp<ROR8>(s1); s2 += dpp<ROR8>(s2);
    float ss = tr32(s1, s2);
    ss = tr16(ss, ss);
    gemv(hn, ac);
    {
      auto sw = __builtin_amdgcn_permlane32_swap(fu(ss), fu(ss), false, false);
      const float t1 = uf(sw[0]), t2 = uf(sw[1]);
      if (lane == 0) { L[stw] = fu(pv + t1 * (1.0f / 80.0f)); L[stw + 8] = fu(t2 - t1 * t1 * (1.0f / 80.0f)); }
    }
    WG_BARRIER();
    if (tw == 0) {
      const float mw = uf(L[str]), qw = uf(L[str + 8]);
      const float G = total();
      const float mean = sum8(mw) * 0.125f;
      const float var = rbf(sum8(qw + 80.0f * (mw - mean) * (mw - mean)) * (1.0f / D));
      const float rinv = __builtin_amdgcn_rcpf(rbf(__builtin_amdgcn_sqrtf(rbf(var + 1e-5f))));
      const float g = (G - rbf(mean) * cw) * rinv;
      auto sa = __builtin_amdgcn_permlane16_swap(fu(g), fu(g), false, false);
      const u32 pr = pk_bf16(uf(sa[0]), uf(sa[1]));
      auto sb = __builtin_amdgcn_permlane32_swap(pr, pr, false, false);
      st_sc1(rs, sadr, u32x4{sb[0], tag, sb[1], tag});
    }
  }
}

extern "C" void kernel_launch(void* const* d_in, const int* in_sizes, int n_in,
                              void* d_out, int out_size, void* d_ws, size_t ws_size,
                              hipStream_t stream) {
  hipLaunchKernelGGL(fused_block, dim3(256), dim3(NTHR), 0, stream,
                     (const u16*)d_in[0], (const u16*)d_in[1], (const u16*)d_in[2], (const u16*)d_in[3], (const u16*)d_in[4],
                     (const u16*)d_in[6], (const u16*)d_in[7], (const u16*)d_in[8], (u16*)d_out, (unsigned char*)d_ws);
}
```
